```python
import torch

def _softmax(x, axis=-1):
    return torch.softmax(x, dim=axis)
def _gelu(x):
    return torch.nn.functional.gelu(x, approximate="tanh")
def _silu(x):
    return torch.nn.functional.silu(x)
def _sigmoid(x):
    return torch.sigmoid(x)
def _layernorm(x, axis=-1, eps=1e-5):
    m = x.mean(dim=axis, keepdim=True)
    v = x.var(dim=axis, keepdim=True, unbiased=False)
    return (x - m) / torch.sqrt(v + eps)
def _scatter_add(tgt, idx, src, axis=0):
    return tgt.clone().index_add_(axis, idx, src)
def _segment_sum(src, seg, n):
    out = torch.zeros((n,) + src.shape[1:], dtype=src.dtype)
    return out.index_add_(0, seg, src)
def _pad(x, pw):
    flat = tuple(v for pair in reversed(pw) for v in pair)
    return torch.nn.functional.pad(x, flat)

kernel_name = 'gated_mix__sliding_block_cached__gqa_block_cached__decode'

def setup_inputs():

    _w = (lambda a: (lambda t: t.to(torch.bfloat16) if t.is_floating_point() else t)(torch.as_tensor(a)))
    def _A_setup():
        B, Sq, D = 2, 1, 576
        Hq, Hkv, Sk, Dh = 32, 16, 8192, 128
        assert Hq % Hkv == 0, (Hq, Hkv)
        return {
            "x": _w((lambda *s: torch.randn(*s, dtype=torch.bfloat16) * 0.05)(B, Sq, D)),
            "k_cache": _w((lambda *s: torch.randn(*s, dtype=torch.bfloat16) * 6.4)(B, Hkv, Sk, Dh)),
            "v_cache": _w((lambda *s: torch.randn(*s, dtype=torch.bfloat16) * 0.05)(B, Hkv, Sk, Dh)),
            "wq": _w((lambda *s: torch.randn(*s, dtype=torch.bfloat16) * 6.4)(D, Hq * Dh) * (D ** -0.5)),
            "wo": _w((lambda *s: torch.randn(*s, dtype=torch.bfloat16) * 0.05)(Hq * Dh, D) * ((Hq * Dh) ** -0.5)),
        }
    _A_in = _A_setup()

    def _B_setup():
        B, Sq, D = 2, 1, 576
        Hq, Hkv, Sk, Dh = 32, 16, 8192, 128
        assert Hq % Hkv == 0, (Hq, Hkv)
        return {
            "x": _w((lambda *s: torch.randn(*s, dtype=torch.bfloat16) * 0.05)(B, Sq, D)),
            "k_cache": _w((lambda *s: torch.randn(*s, dtype=torch.bfloat16) * 6.4)(B, Hkv, Sk, Dh)),
            "v_cache": _w((lambda *s: torch.randn(*s, dtype=torch.bfloat16) * 0.05)(B, Hkv, Sk, Dh)),
            "wq": _w((lambda *s: torch.randn(*s, dtype=torch.bfloat16) * 6.4)(D, Hq * Dh) * (D ** -0.5)),
            "wo": _w((lambda *s: torch.randn(*s, dtype=torch.bfloat16) * 0.05)(Hq * Dh, D) * ((Hq * Dh) ** -0.5)),
        }
    _B_in = _B_setup()

    return {
        "A_x": _A_in["x"],
        "A_k_cache": _A_in["k_cache"],
        "A_v_cache": _A_in["v_cache"],
        "A_wq": _A_in["wq"],
        "A_wo": _A_in["wo"],
        "B_x": _B_in["x"],
        "B_k_cache": _B_in["k_cache"],
        "B_v_cache": _B_in["v_cache"],
        "B_wq": _B_in["wq"],
        "B_wo": _B_in["wo"],
        "gate_w": _w((lambda *s: torch.randn(*s, dtype=torch.bfloat16) * 0.05)(576) * 16.0),
    }

def reference(A_x, A_k_cache, A_v_cache, A_wq, A_wo, B_x, B_k_cache, B_v_cache, B_wq, B_wo, gate_w):
    def _A_ref(x, k_cache, v_cache, wq, wo):
        B, Sq, D = x.shape
        Hq, Dh = 32, 128
        Hkv, Sk = k_cache.shape[1], k_cache.shape[2]
        W = min(4096, max(256, Sk // 4))
        rep = Hq // Hkv
        q = (lambda x, *d: x.permute(*d))((x @ wq).reshape(B, Sq, Hq, Dh), 0, 2, 1, 3)
        k = (lambda x, n, axis: torch.repeat_interleave(x, n, dim=axis))(k_cache[:, :, Sk - W:, :], rep, axis=1)
        v = (lambda x, n, axis: torch.repeat_interleave(x, n, dim=axis))(v_cache[:, :, Sk - W:, :], rep, axis=1)
        scores = torch.einsum("bhqd,bhkd->bhqk", q, k) * (Dh ** -0.5)
        p = _softmax(scores, axis=-1)
        o = torch.einsum("bhqk,bhkd->bhqd", p, v)
        return (lambda x, *d: x.permute(*d))(o, 0, 2, 1, 3).reshape(B, Sq, Hq * Dh) @ wo

    def _B_ref(x, k_cache, v_cache, wq, wo):
        B, Sq, D = x.shape
        Hq, Dh = 32, 128
        Hkv = k_cache.shape[1]
        rep = Hq // Hkv
        q = (lambda x, *d: x.permute(*d))((x @ wq).reshape(B, Sq, Hq, Dh), 0, 2, 1, 3)
        k = (lambda x, n, axis: torch.repeat_interleave(x, n, dim=axis))(k_cache, rep, axis=1)
        v = (lambda x, n, axis: torch.repeat_interleave(x, n, dim=axis))(v_cache, rep, axis=1)
        scores = torch.einsum("bhqd,bhkd->bhqk", q, k) * (Dh ** -0.5)
        p = _softmax(scores, axis=-1)
        o = torch.einsum("bhqk,bhkd->bhqd", p, v)
        return (lambda x, *d: x.permute(*d))(o, 0, 2, 1, 3).reshape(B, Sq, Hq * Dh) @ wo

    x = A_x
    g = _sigmoid((x * gate_w).sum(-1, keepdims=True))
    a = _A_ref(x, A_k_cache, A_v_cache, A_wq, A_wo)
    b = _B_ref(x, B_k_cache, B_v_cache, B_wq, B_wo)
    return g * a + (1 - g) * b
```

```cpp
#include <hip/hip_runtime.h>
#include <hip/hip_bf16.h>
#include <stdint.h>

#define GAS __attribute__((address_space(1)))
typedef unsigned v4u __attribute__((ext_vector_type(4)));
typedef float f32x4 __attribute__((ext_vector_type(4)));
typedef short bf16x8 __attribute__((ext_vector_type(8)));
typedef __bf16 bf16x2 __attribute__((ext_vector_type(2)));

constexpr int NB = 2, DM = 576, HQ = 32, HKV = 16, SK = 8192, DH = 128, WIN = 2048;
constexpr int NSPLIT = 8;

__device__ __forceinline__ v4u ldnt(const void* p) { return __builtin_nontemporal_load((const GAS v4u*)p); }
__device__ __forceinline__ f32x4 mfma(v4u a, v4u b, f32x4 c) {
  return __builtin_amdgcn_mfma_f32_16x16x32_bf16(__builtin_bit_cast(bf16x8, a), __builtin_bit_cast(bf16x8, b), c, 0, 0, 0);
}
__device__ __forceinline__ float bf2f(unsigned short u) { return __builtin_bit_cast(float, (unsigned)u << 16); }
__device__ __forceinline__ unsigned cvtpk(float lo, float hi) {
  bf16x2 r; r.x = (__bf16)lo; r.y = (__bf16)hi; return __builtin_bit_cast(unsigned, r);
}
__device__ __forceinline__ float xor16_32_max(float x) {
  x = fmaxf(x, __shfl_xor(x, 16)); x = fmaxf(x, __shfl_xor(x, 32)); return x;
}
__device__ __forceinline__ float xor16_32_sum(float x) {
  x += __shfl_xor(x, 16); x += __shfl_xor(x, 32); return x;
}

__global__ void __launch_bounds__(256) k_qproj(const unsigned short* __restrict__ x, const unsigned short* __restrict__ wqA,
                                               const unsigned short* __restrict__ wqB, unsigned short* __restrict__ q) {
  __shared__ float red[4][2][32];
  const int w = blockIdx.x, br = w >> 7, c0 = (w & 127) * 32;
  const unsigned short* wq = br ? wqB : wqA;
  const int tid = threadIdx.x, lane = tid & 63, wave = tid >> 6;
  const int ch = tid & 3, r0 = tid >> 2;
  float acc[2][8];
#pragma unroll
  for (int b = 0; b < 2; ++b)
#pragma unroll
    for (int j = 0; j < 8; ++j) acc[b][j] = 0.f;
  v4u wv[9];
#pragma unroll
  for (int i = 0; i < 9; ++i) wv[i] = ldnt(wq + (size_t)(r0 + 64 * i) * 4096 + c0 + ch * 8);
#pragma unroll
  for (int i = 0; i < 9; ++i) {
    const float x0 = bf2f(x[r0 + 64 * i]), x1 = bf2f(x[DM + r0 + 64 * i]);
#pragma unroll
    for (int k = 0; k < 4; ++k) {
      const float lo = __builtin_bit_cast(float, wv[i][k] << 16), hi = __builtin_bit_cast(float, wv[i][k] & 0xffff0000u);
      acc[0][2 * k] = fmaf(x0, lo, acc[0][2 * k]); acc[0][2 * k + 1] = fmaf(x0, hi, acc[0][2 * k + 1]);
      acc[1][2 * k] = fmaf(x1, lo, acc[1][2 * k]); acc[1][2 * k + 1] = fmaf(x1, hi, acc[1][2 * k + 1]);
    }
  }
#pragma unroll
  for (int b = 0; b < 2; ++b)
#pragma unroll
    for (int j = 0; j < 8; ++j) {
      float v = acc[b][j];
      v += __shfl_xor(v, 4); v += __shfl_xor(v, 8); v += __shfl_xor(v, 16); v += __shfl_xor(v, 32);
      if (lane < 4) red[wave][b][ch * 8 + j] = v;
    }
  __syncthreads();
  if (tid < 64) {
    const int b = tid >> 5, c = tid & 31;
    const float s = (red[0][b][c] + red[1][b][c]) + (red[2][b][c] + red[3][b][c]);
    q[(br * 2 + b) * 4096 + c0 + c] = (unsigned short)(cvtpk(s, 0.f) & 0xffff);
  }
}

template <int NT>
__device__ __forceinline__ void attn_wave(const unsigned short* kb, const unsigned short* vb, const v4u (&qf)[4], int lane,
                                          const v4u& msk, float& m_out, float& l_out, f32x4& o_out) {
  constexpr float C2 = 0.08838834764831845f * 1.4426950408889634f;
  const int g = lane >> 4, r = lane & 15;
  const unsigned short* kp = kb + r * DH + g * 8;
  const unsigned short* vp = vb + (4 * g) * DH + r * 8;
  v4u kx[NT][4];
#pragma unroll
  for (int t = 0; t < NT; ++t)
#pragma unroll
    for (int st = 0; st < 4; ++st) kx[t][st] = ldnt(kp + t * 16 * DH + st * 32);
  __builtin_amdgcn_sched_barrier(0);
  f32x4 s[NT];
  v4u vx[NT][4];
#pragma unroll
  for (int t = 0; t < NT; ++t) {
    f32x4 d = {0.f, 0.f, 0.f, 0.f};
#pragma unroll
    for (int st = 0; st < 4; ++st) d = mfma(kx[t][st], qf[st], d);
    s[t] = d;
#pragma unroll
    for (int i = 0; i < 4; ++i) vx[t][i] = ldnt(vp + t * 16 * DH + i * DH);
    __builtin_amdgcn_sched_barrier(0);
  }
  float m = -INFINITY;
#pragma unroll
  for (int t = 0; t < NT; ++t) m = fmaxf(fmaxf(fmaxf(s[t][0], s[t][1]), fmaxf(s[t][2], s[t][3])), m);
  m = xor16_32_max(m);
  const float mc = m * C2;
  float l = 0.f;
  unsigned pp[NT][4];
#pragma unroll
  for (int t = 0; t < NT; ++t)
#pragma unroll
    for (int i = 0; i < 4; ++i) {
      const float p = __builtin_amdgcn_exp2f(fmaf(s[t][i], C2, -mc));
      l += p;
      pp[t][i] = cvtpk(p, p);
    }
  l = xor16_32_sum(l);
  f32x4 o = {0.f, 0.f, 0.f, 0.f};
#pragma unroll
  for (int t = 0; t < NT; ++t) {
#pragma unroll
    for (int i = 0; i < 4; ++i) {
      v4u a = {pp[t][i] & msk[0], pp[t][i] & msk[1], pp[t][i] & msk[2], pp[t][i] & msk[3]};
      o = mfma(a, vx[t][i], o);
    }
    __builtin_amdgcn_sched_barrier(0);
  }
  m_out = mc; l_out = l; o_out = o;
}

__global__ void __launch_bounds__(512) k_attn(const unsigned short* __restrict__ kA, const unsigned short* __restrict__ vA,
                                              const unsigned short* __restrict__ kB, const unsigned short* __restrict__ vB,
                                              const unsigned short* __restrict__ q, float* __restrict__ po, float* __restrict__ pml) {
  __shared__ float sm_o[2][8][256];
  __shared__ float sm_ml[2][8][2][2];
  const int w = blockIdx.x;
  const int pair = w >> 3, s = w & 7, b = pair >> 4, kvh = pair & 15;
  const int tid = threadIdx.x, lane = tid & 63, wave = __builtin_amdgcn_readfirstlane(tid >> 6);
  const int g = lane >> 4, n = lane & 15, h = n >> 3, j0 = n & 7;
  v4u msk;
#pragma unroll
  for (int i = 0; i < 4; ++i) msk[i] = ((j0 >> 1) == i) ? ((j0 & 1) ? 0xffff0000u : 0x0000ffffu) : 0u;
  const size_t base = (size_t)pair * SK * DH;
  v4u qa[4], qb[4];
#pragma unroll
  for (int st = 0; st < 4; ++st) {
    qa[st] = *(const v4u*)(q + (0 * 2 + b) * 4096 + (kvh * 2 + h) * DH + st * 32 + g * 8);
    qb[st] = *(const v4u*)(q + (1 * 2 + b) * 4096 + (kvh * 2 + h) * DH + st * 32 + g * 8);
  }
  float m, l; f32x4 o;
  {
    const size_t off = base + (size_t)(SK - WIN + s * 256 + wave * 32) * DH;
    attn_wave<2>(kA + off, vA + off, qa, lane, msk, m, l, o);
    *(f32x4*)&sm_o[0][wave][(g >> 1) * 128 + n * 8 + (g & 1) * 4] = o;
    if (g == 0 && j0 == 0) { sm_ml[0][wave][h][0] = m; sm_ml[0][wave][h][1] = l; }
  }
  {
    const size_t off = base + (size_t)(s * 1024 + wave * 128) * DH;
    attn_wave<8>(kB + off, vB + off, qb, lane, msk, m, l, o);
    *(f32x4*)&sm_o[1][wave][(g >> 1) * 128 + n * 8 + (g & 1) * 4] = o;
    if (g == 0 && j0 == 0) { sm_ml[1][wave][h][0] = m; sm_ml[1][wave][h][1] = l; }
  }
  __syncthreads();
  {
    const int br = tid >> 8, hh = (tid >> 7) & 1, d = tid & 127;
    float M = -INFINITY;
#pragma unroll
    for (int ww = 0; ww < 8; ++ww) M = fmaxf(M, sm_ml[br][ww][hh][0]);
    float L = 0.f, O = 0.f;
#pragma unroll
    for (int ww = 0; ww < 8; ++ww) {
      const float f = __builtin_amdgcn_exp2f(sm_ml[br][ww][hh][0] - M);
      L = fmaf(f, sm_ml[br][ww][hh][1], L);
      O = fmaf(f, sm_o[br][ww][hh * 128 + d], O);
    }
    const size_t row = ((size_t)(br * 2 + b) * HQ + kvh * 2 + hh) * NSPLIT + s;
    po[row * 128 + d] = O;
    if (d == 0) { pml[row * 2] = M; pml[row * 2 + 1] = L; }
  }
}

__global__ void __launch_bounds__(320) k_proj(const float* __restrict__ po, const float* __restrict__ pml,
                                              const unsigned short* __restrict__ woA, const unsigned short* __restrict__ woB,
                                              float* __restrict__ part) {
  __shared__ unsigned sm_p[2][32];
  const int w = blockIdx.x, br = w >> 7, r0 = (w & 127) * 32;
  const unsigned short* wo = br ? woB : woA;
  const int tid = threadIdx.x, lane = tid & 63, wave = __builtin_amdgcn_readfirstlane(tid >> 6);
  const int g = lane >> 4, n = lane & 15, h = n >> 3, j0 = n & 7;
  v4u wv[8];
  const bool act = (wave < 4) || (n < 8);
#pragma unroll
  for (int i = 0; i < 8; ++i) {
    wv[i] = v4u{0u, 0u, 0u, 0u};
    if (act) wv[i] = ldnt(wo + (size_t)(r0 + 4 * i + g) * DM + wave * 128 + n * 8);
  }
  if (tid < 64) {
    const int b = tid >> 5, j = r0 + (tid & 31), hq = j >> 7, d = j & 127;
    const size_t row = ((size_t)(br * 2 + b) * HQ + hq) * NSPLIT;
    float M = -INFINITY;
#pragma unroll
    for (int s = 0; s < NSPLIT; ++s) M = fmaxf(M, pml[(row + s) * 2]);
    float L = 0.f, O = 0.f;
#pragma unroll
    for (int s = 0; s < NSPLIT; ++s) {
      const float f = __builtin_amdgcn_exp2f(pml[(row + s) * 2] - M);
      L = fmaf(f, pml[(row + s) * 2 + 1], L);
      O = fmaf(f, po[(row + s) * 128 + d], O);
    }
    const float o = O / L;
    sm_p[b][tid & 31] = cvtpk(o, o);
  }
  __syncthreads();
  v4u msk;
#pragma unroll
  for (int i = 0; i < 4; ++i) msk[i] = ((j0 >> 1) == i) ? ((j0 & 1) ? 0xffff0000u : 0x0000ffffu) : 0u;
  f32x4 o = {0.f, 0.f, 0.f, 0.f};
#pragma unroll
  for (int i = 0; i < 8; ++i) {
    const unsigned pp = sm_p[h][4 * i + g];
    v4u a = {pp & msk[0], pp & msk[1], pp & msk[2], pp & msk[3]};
    o = mfma(a, wv[i], o);
  }
  if (act) *(f32x4*)&part[(size_t)w * 1152 + (g >> 1) * DM + wave * 128 + n * 8 + (g & 1) * 4] = o;
}

__global__ void __launch_bounds__(256) k_fin(const float* __restrict__ part, const unsigned short* __restrict__ x,
                                             const unsigned short* __restrict__ gw, unsigned short* __restrict__ out) {
  __shared__ float red[2][16][16];
  __shared__ float gs[4];
  const int w = blockIdx.x, tid = threadIdx.x, lane = tid & 63, wave = tid >> 6;
  const int o0 = w * 16, b = o0 / DM;
  const int c = tid & 15, p0 = tid >> 4;
  float sa = 0.f, sb = 0.f;
#pragma unroll
  for (int i = 0; i < 8; ++i) {
    sa += part[(size_t)(p0 + 16 * i) * 1152 + o0 + c];
    sb += part[(size_t)(128 + p0 + 16 * i) * 1152 + o0 + c];
  }
  red[0][p0][c] = sa; red[1][p0][c] = sb;
  float gsum = 0.f;
  for (int i = tid; i < DM; i += 256) gsum += bf2f(x[b * DM + i]) * bf2f(gw[i]);
#pragma unroll
  for (int k = 1; k < 64; k <<= 1) gsum += __shfl_xor(gsum, k);
  if (lane == 0) gs[wave] = gsum;
  __syncthreads();
  if (tid < 16) {
    float a = 0.f, bb = 0.f;
#pragma unroll
    for (int i = 0; i < 16; ++i) { a += red[0][i][tid]; bb += red[1][i][tid]; }
    const float z = (gs[0] + gs[1]) + (gs[2] + gs[3]);
    const float gt = 1.f / (1.f + __expf(-z));
    const float r = gt * a + (1.f - gt) * bb;
    out[o0 + tid] = (unsigned short)(cvtpk(r, 0.f) & 0xffff);
  }
}

extern "C" void kernel_launch(void* const* d_in, const int* in_sizes, int n_in, void* d_out, int out_size, void* d_ws, size_t ws_size,
                              hipStream_t stream) {
  const unsigned short* x = (const unsigned short*)d_in[0];
  const unsigned short* kA = (const unsigned short*)d_in[1];
  const unsigned short* vA = (const unsigned short*)d_in[2];
  const unsigned short* wqA = (const unsigned short*)d_in[3];
  const unsigned short* woA = (const unsigned short*)d_in[4];
  const unsigned short* kB = (const unsigned short*)d_in[6];
  const unsigned short* vB = (const unsigned short*)d_in[7];
  const unsigned short* wqB = (const unsigned short*)d_in[8];
  const unsigned short* woB = (const unsigned short*)d_in[9];
  const unsigned short* gw = (const unsigned short*)d_in[10];
  char* ws = (char*)d_ws;
  unsigned short* q = (unsigned short*)ws;
  float* pml = (float*)(ws + 65536);
  float* po = (float*)(ws + 131072);
  float* part = (float*)(ws + 1048576);
  k_qproj<<<256, 256, 0, stream>>>(x, wqA, wqB, q);
  k_attn<<<256, 512, 0, stream>>>(kA, vA, kB, vB, q, po, pml);
  k_proj<<<256, 320, 0, stream>>>(po, pml, woA, woB, part);
  k_fin<<<72, 256, 0, stream>>>(part, x, gw, (unsigned short*)d_out);
}
```

```cpp
#include <hip/hip_runtime.h>
#include <stdint.h>

#define W_INFL 3
#define N_FILL 20
#define STAMPS 0

#define GAS __attribute__((address_space(1)))
#define LAS __attribute__((address_space(3)))
typedef unsigned v4u __attribute__((ext_vector_type(4)));
typedef unsigned v2u __attribute__((ext_vector_type(2)));
typedef float f32x4 __attribute__((ext_vector_type(4)));
typedef short bf16x8 __attribute__((ext_vector_type(8)));
typedef __bf16 bf16x2 __attribute__((ext_vector_type(2)));
typedef __amdgpu_buffer_rsrc_t rsrc_t;

constexpr int DM = 576, SK = 8192, DH = 128, WIN = 2048;
constexpr int NTHR = 576;
constexpr int AUX_NT = 2, AUX_SC1 = 16;
constexpr unsigned SPIN_CAP = 1u << 16;
constexpr int WS_EPOCH = 0;
constexpr int WS_QX = 4096;
constexpr int WS_OX = WS_QX + 256 * 1024;
constexpr int WS_ML = WS_OX + 256 * 2048;
constexpr int WS_PX = WS_ML + 256 * 128;
constexpr int WS_END = WS_PX + 256 * 4608;
constexpr int WS_STAMP = 4 * 1024 * 1024;
constexpr int L_QP = 0;
constexpr int L_Q = L_QP + 9 * 1024;
constexpr int L_OP = L_Q + 1024;
constexpr int L_ML = L_OP + 16384;
constexpr int L_OF = L_ML + 256;
constexpr int L_RED = L_OF + 256;
constexpr int L_FLAG = L_RED + 1024;
constexpr int L_END = L_FLAG + 64;
constexpr int LDS_BYTES = 84 * 1024;
static_assert(L_END <= LDS_BYTES, "lds");

__device__ __forceinline__ rsrc_t mk(const void* p, int bytes) { return __builtin_amdgcn_make_buffer_rsrc((void*)p, 0, bytes, 0x00020000); }
template <int AUX> __device__ __forceinline__ v4u ld128(rsrc_t r, int vo, int so) { return __builtin_amdgcn_raw_buffer_load_b128(r, vo, so, AUX); }
__device__ __forceinline__ f32x4 mfma(v4u a, v4u b, f32x4 c) {
  return __builtin_amdgcn_mfma_f32_16x16x32_bf16(__builtin_bit_cast(bf16x8, a), __builtin_bit_cast(bf16x8, b), c, 0, 0, 0);
}
__device__ __forceinline__ float u2f(unsigned u) { return __builtin_bit_cast(float, u); }
__device__ __forceinline__ unsigned f2u(float f) { return __builtin_bit_cast(unsigned, f); }
__device__ __forceinline__ unsigned cvtpk(float lo, float hi) {
  bf16x2 r; r.x = (__bf16)lo; r.y = (__bf16)hi; return __builtin_bit_cast(unsigned, r);
}
__device__ __forceinline__ float rbf(float x) { return u2f(cvtpk(x, x) & 0xffff0000u); }
template <int CTRL> __device__ __forceinline__ unsigned dppu(unsigned x) { return (unsigned)__builtin_amdgcn_mov_dpp((int)x, CTRL, 0xf, 0xf, true); }
template <int CTRL> __device__ __forceinline__ float dpp(float x) { return u2f(dppu<CTRL>(f2u(x))); }
constexpr int XOR1 = 0xB1, XOR2 = 0x4E, XOR3 = 0x1B, ROR8 = 0x128, ROR4 = 0x124;
__device__ __forceinline__ float x16_max(float x) { auto s = __builtin_amdgcn_permlane16_swap(f2u(x), f2u(x), false, false); return fmaxf(u2f(s[0]), u2f(s[1])); }
__device__ __forceinline__ float x32_max(float x) { auto s = __builtin_amdgcn_permlane32_swap(f2u(x), f2u(x), false, false); return fmaxf(u2f(s[0]), u2f(s[1])); }
__device__ __forceinline__ float x16_sum(float x) { auto s = __builtin_amdgcn_permlane16_swap(f2u(x), f2u(x), false, false); return u2f(s[0]) + u2f(s[1]); }
__device__ __forceinline__ float x32_sum(float x) { auto s = __builtin_amdgcn_permlane32_swap(f2u(x), f2u(x), false, false); return u2f(s[0]) + u2f(s[1]); }
__device__ __forceinline__ float t16_sum(float a, float b) { auto s = __builtin_amdgcn_permlane16_swap(f2u(a), f2u(b), false, false); return u2f(s[0]) + u2f(s[1]); }
__device__ __forceinline__ float t32_sum(float a, float b) { auto s = __builtin_amdgcn_permlane32_swap(f2u(a), f2u(b), false, false); return u2f(s[0]) + u2f(s[1]); }

__device__ __forceinline__ v4u tag4(f32x4 v, unsigned T) {
  return v4u{(f2u(v[0]) & 0xffffff00u) | T, (f2u(v[1]) & 0xffffff00u) | T, (f2u(v[2]) & 0xffffff00u) | T, (f2u(v[3]) & 0xffffff00u) | T};
}
__device__ __forceinline__ unsigned bad4(v4u d, unsigned T) { return ((d[0] ^ T) | (d[1] ^ T) | (d[2] ^ T) | (d[3] ^ T)) & 0xffu; }
__device__ __forceinline__ f32x4 untag4(v4u d) {
  return f32x4{u2f(d[0] & 0xffffff00u), u2f(d[1] & 0xffffff00u), u2f(d[2] & 0xffffff00u), u2f(d[3] & 0xffffff00u)};
}
#define VMWAIT(n) __builtin_amdgcn_s_waitcnt(((n) & 15) | (((n) >> 4) << 14) | 0x0F70)
#define LGKM0() asm volatile("s_waitcnt lgkmcnt(0)" ::: "memory")
#define SB() __builtin_amdgcn_sched_barrier(0)
#define PINV(x) asm volatile("" : "+v"(x))
#define PINS(x) asm volatile("" : "+s"(x))
constexpr int OOB = 0x40000000;
__device__ __forceinline__ void wg_barrier() { LGKM0(); __builtin_amdgcn_s_barrier(); asm volatile("" ::: "memory"); }
__device__ __forceinline__ void lds_wait_ge(volatile LAS unsigned* p, unsigned want) {
#pragma nounroll
  for (unsigned spins = 0; spins < (SPIN_CAP << 4); ++spins) {
    if ((unsigned)__builtin_amdgcn_readfirstlane(*p) >= want) break;
    __builtin_amdgcn_s_sleep(1);
  }
  asm volatile("" ::: "memory");
}
#define STAMP(i) do {} while (0)
#define STAMP_OUT() do {} while (0)

template <int A> __device__ __forceinline__ v4u afrag(unsigned pp, const v4u& msk) {
  const unsigned v = dppu<A * 0x55>(pp);
  return v4u{v & msk[0], v & msk[1], v & msk[2], v & msk[3]};
}

struct Stream {
  rsrc_t kA, kB, vA, vB, wo;
  int kvo, vvo, ovo;
};
template <int K> __device__ __forceinline__ v4u issue(const Stream& S) {
  if constexpr (K < 8) return ld128<AUX_NT>(S.kA, S.kvo, (K >> 2) * 32768 + (K & 3) * 1024);
  else if constexpr (K < 40) return ld128<AUX_NT>(S.kB, S.kvo, ((K - 8) >> 2) * 32768 + (K & 3) * 1024);
  else if constexpr (K < 48) return ld128<AUX_NT>(S.vA, S.vvo, ((K - 40) >> 2) * 32768 + (K & 3) * 1024);
  else if constexpr (K < 80) return ld128<AUX_NT>(S.vB, S.vvo, ((K - 48) >> 2) * 32768 + (K & 3) * 1024);
  else return ld128<AUX_NT>(S.wo, S.ovo, (K - 80) * 8 * 1152);
}

struct State {
  v4u buf[84];
  v4u q[2];
  v4u msk;
  f32x4 d[4];
  float sc[10];
  unsigned pp[10];
  f32x4 acc[2];
  bool p1, p2;
};

template <int LO, int HI> __device__ __forceinline__ void softmax(State& st, LAS unsigned char* L, int br, int wave, int lane) {
  float m = st.sc[LO];
#pragma unroll
  for (int t = LO + 1; t < HI; ++t) m = fmaxf(m, st.sc[t]);
  m = fmaxf(m, dpp<XOR1>(m)); m = fmaxf(m, dpp<XOR2>(m)); m = x16_max(m); m = x32_max(m);
  float l = 0.f;
#pragma unroll
  for (int t = LO; t < HI; ++t) {
    const float p = __builtin_amdgcn_exp2f((st.sc[t] - m) * 1.4426950408889634f);
    l += p;
    st.pp[t] = cvtpk(p, p);
  }
  l += dpp<XOR1>(l); l += dpp<XOR2>(l); l = x16_sum(l); l = x32_sum(l);
  if ((lane & 0x37) == 0) *(LAS v2u*)(L + L_ML + ((br * 2 + (lane >> 3)) * 8 + wave) * 8) = v2u{f2u(m), f2u(l)};
}

template <int K> __device__ __forceinline__ void process(State& st, LAS unsigned char* L, int wave, int lane) {
  if constexpr (K < 40) {
    constexpr int a = K & 3, t = K >> 2, br = K < 8 ? 0 : 1;
    st.d[a] = mfma(st.buf[K], st.q[br], f32x4{0.f, 0.f, 0.f, 0.f});
    if constexpr (a == 3) {
      float w[4];
#pragma unroll
      for (int x = 0; x < 4; ++x) {
        const float lo = st.p1 ? st.d[1 ^ x][1] : st.d[0 ^ x][0];
        const float hi = st.p1 ? st.d[3 ^ x][3] : st.d[2 ^ x][2];
        w[x] = st.p2 ? hi : lo;
      }
      const float r = (w[0] + dpp<XOR1>(w[1])) + (dpp<XOR2>(w[2]) + dpp<XOR3>(w[3]));
      st.sc[t] = rbf(rbf(r) * 0.08838834764831845f);
      if constexpr (K == 7) softmax<0, 2>(st, L, 0, wave, lane);
      if constexpr (K == 39) softmax<2, 10>(st, L, 1, wave, lane);
    }
  } else {
    constexpr int a = K & 3, t = (K - 40) >> 2, br = K < 48 ? 0 : 1;
    st.acc[br] = mfma(afrag<a>(st.pp[t], st.msk), st.buf[K], st.acc[br]);
  }
}

template <int K, int HI> __device__ __forceinline__ void fill(State& st, const Stream& S) {
  if constexpr (K < HI) {
    VMWAIT(W_INFL - 1);
    st.buf[K] = issue<K>(S);
    SB();
    fill<K + 1, HI>(st, S);
  }
}
template <int P, int HI> __device__ __forceinline__ void proc_range(State& st, LAS unsigned char* L, int wave, int lane) {
  if constexpr (P < HI) {
    process<P>(st, L, wave, lane);
    proc_range<P + 1, HI>(st, L, wave, lane);
  }
}
constexpr int cmin(int a, int b) { return a < b ? a : b; }
template <int I, int P> __device__ __forceinline__ void mainloop(State& st, const Stream& S, LAS unsigned char* L, int wave, int lane) {
  if constexpr (I < 84) {
    VMWAIT(W_INFL - 1);
    st.buf[I] = issue<I>(S);
    SB();
    constexpr int PH = cmin(cmin(P + 4, I + 1 - W_INFL), 80);
    proc_range<P, PH>(st, L, wave, lane);
    SB();
    mainloop<I + 1, PH>(st, S, L, wave, lane);
  } else if constexpr (P < 80) {
    process<P>(st, L, wave, lane);
    SB();
    mainloop<I, P + 1>(st, S, L, wave, lane);
  }
}

__global__ void __launch_bounds__(NTHR) fused_decode(const unsigned short* __restrict__ x, const unsigned short* __restrict__ kA,
    const unsigned short* __restrict__ vA, const unsigned short* __restrict__ wqA, const unsigned short* __restrict__ woA,
    const unsigned short* __restrict__ kB, const unsigned short* __restrict__ vB, const unsigned short* __restrict__ wqB,
    const unsigned short* __restrict__ woB, const unsigned short* __restrict__ gw, unsigned short* __restrict__ out, unsigned char* ws) {
  __shared__ __attribute__((aligned(16))) unsigned char smem[LDS_BYTES];
  LAS unsigned char* L = (LAS unsigned char*)smem;
  volatile LAS unsigned* flag = (volatile LAS unsigned*)(L + L_FLAG);
  const int w = blockIdx.x, pair = w >> 3, s = w & 7, b = pair >> 4, kvh = pair & 15, mi = b * 8 + s;
  const int tid = threadIdx.x, lane = tid & 63, wave = __builtin_amdgcn_readfirstlane(tid >> 6);
  const int g = lane >> 4, n = lane & 15, j0 = n & 7;
  STAMP(0);
  const rsrc_t rws = mk(ws, WS_END);
  const int phead = kvh * 2 + (s >> 2), pq = s & 3;
  const rsrc_t rwq = mk((b ? wqB : wqA) + (size_t)(pq * 144) * 4096 + phead * DH, 144 * 8192);
  const rsrc_t rx = mk(x, 2 * DM * 2);
  const unsigned ep = __builtin_amdgcn_raw_buffer_load_b32(rws, 0, WS_EPOCH, AUX_SC1);
  const v2u xr = __builtin_amdgcn_raw_buffer_load_b64(rx, ((n >> 3) * DM + pq * 144 + wave * 16 + g * 4) * 2, 0, 0);
  v4u wq[4];
#pragma unroll
  for (int i = 0; i < 4; ++i) wq[i] = ld128<AUX_NT>(rwq, (wave * 16 + g * 4) * 8192 + n * 16, i * 8192);
  SB();
  if (tid < 4) flag[tid] = 0u;
  State st;
#pragma unroll
  for (int i = 0; i < 4; ++i) st.msk[i] = ((j0 >> 1) == i) ? ((j0 & 1) ? 0xffff0000u : 0x0000ffffu) : 0u;
  st.p1 = lane & 1; st.p2 = lane & 2;
  Stream S;
  {
    const size_t base = (size_t)pair * SK * DH;
    const size_t offA = base + (size_t)(SK - WIN + s * 256) * DH, offB = base + (size_t)(s * 1024) * DH;
    S.kA = mk(kA + offA, 65536); S.vA = mk(vA + offA, 65536);
    S.kB = mk(kB + offB, 262144); S.vB = mk(vB + offB, 262144);
    S.wo = mk((b ? woB : woA) + (size_t)(phead * DH + pq * 32) * DM, 32 * 1152);
    S.kvo = wave * 4096 + (n >> 2) * 256 + g * 64 + (n & 3) * 16;
    S.vvo = wave * 4096 + lane * 16;
    S.ovo = (g * 2 + (n >> 3)) * 1152 + wave * 128 + j0 * 16;
  }
  wg_barrier();
  auto qpartial = [&]() {
    f32x4 qd = {0.f, 0.f, 0.f, 0.f};
#pragma unroll
    for (int i = 0; i < 4; ++i) {
      const unsigned xx = __builtin_amdgcn_perm(xr[i >> 1], xr[i >> 1], (i & 1) ? 0x03020302u : 0x01000100u);
      qd = mfma(v4u{xx & st.msk[0], xx & st.msk[1], xx & st.msk[2], xx & st.msk[3]}, wq[i], qd);
    }
    *(LAS f32x4*)(L + L_QP + wave * 1024 + lane * 16) = qd;
    LGKM0();
    if (lane == 0) __hip_atomic_fetch_add((LAS unsigned*)(L + L_FLAG), 1u, __ATOMIC_RELAXED, __HIP_MEMORY_SCOPE_WORKGROUP);
    STAMP(1);
    SB();
  };
  const int dpos = (g >> 1) * 512 + n * 32 + (g & 1) * 16;
  auto combine = [&](const int br, const unsigned T) {
    const int h = g >> 1;
    f32x4 ml[4];
#pragma unroll
    for (int i = 0; i < 4; ++i) ml[i] = *(LAS f32x4*)(L + L_ML + (br * 2 + h) * 64 + i * 16);
    f32x4 op[8];
#pragma unroll
    for (int i = 0; i < 8; ++i) op[i] = *(LAS f32x4*)(L + L_OP + br * 8192 + i * 1024 + lane * 16);
    float M = fmaxf(ml[0][0], ml[0][2]);
#pragma unroll
    for (int i = 1; i < 4; ++i) M = fmaxf(M, fmaxf(ml[i][0], ml[i][2]));
    float Ls = 0.f; f32x4 O = {0.f, 0.f, 0.f, 0.f};
#pragma unroll
    for (int i = 0; i < 8; ++i) {
      const float f = __builtin_amdgcn_exp2f((ml[i >> 1][(i & 1) * 2] - M) * 1.4426950408889634f);
      Ls = fmaf(f, ml[i >> 1][(i & 1) * 2 + 1], Ls);
      O += f * op[i];
    }
    __builtin_amdgcn_raw_buffer_store_b128(tag4(O, T), rws, dpos, WS_OX + (kvh * 16 + mi) * 2048 + br * 1024, AUX_SC1);
    __builtin_amdgcn_raw_buffer_store_b128(tag4(f32x4{M, Ls, 0.f, 0.f}, T), rws, (lane & 31) ? OOB : h * 16, WS_ML + (kvh * 16 + mi) * 128 + br * 32, AUX_SC1);
  };
  auto project = [&](const v4u wo0, const v4u wo1, const v4u wo2, const v4u wo3, const unsigned T) {
    const v4u f0 = *(LAS v4u*)(L + L_OF + ((n >> 3) * 32 + g * 8) * 4), f1 = *(LAS v4u*)(L + L_OF + ((n >> 3) * 32 + g * 8) * 4 + 16);
    const v4u msk = st.msk;
    f32x4 d0 = {0.f, 0.f, 0.f, 0.f}, d1 = {0.f, 0.f, 0.f, 0.f};
#define PROJ(WV, A0, A1) d0 = mfma(v4u{A0 & msk[0], A0 & msk[1], A0 & msk[2], A0 & msk[3]}, WV, d0); \
                         d1 = mfma(v4u{A1 & msk[0], A1 & msk[1], A1 & msk[2], A1 & msk[3]}, WV, d1);
    PROJ(wo0, f0[0], f0[1]) PROJ(wo1, f0[2], f0[3]) PROJ(wo2, f1[0], f1[1]) PROJ(wo3, f1[2], f1[3])
#undef PROJ
    f32x4 y;
#pragma unroll
    for (int i = 0; i < 4; ++i) y[i] = d0[i] + dpp<ROR8>(d1[i]);
    __builtin_amdgcn_raw_buffer_store_b128(tag4(y, T), rws, n < 8 ? (g >> 1) * 2304 + wave * 256 + n * 32 + (g & 1) * 16 : OOB, WS_PX + w * 4608, AUX_SC1);
  };

  if (wave < 8) {
    fill<0, W_INFL>(st, S);
    const unsigned T = ((unsigned)__builtin_amdgcn_readfirstlane(ep) + 1u) & 0xffu;
    qpartial();
    fill<W_INFL, N_FILL>(st, S);
    STAMP(2);
    lds_wait_ge(flag + 1, 1u);
    STAMP(3);
    st.q[0] = *(LAS v4u*)(L + L_Q + (n >> 3) * 256 + (g * 4 + (n & 3)) * 16);
    st.q[1] = *(LAS v4u*)(L + L_Q + 512 + (n >> 3) * 256 + (g * 4 + (n & 3)) * 16);
    st.acc[0] = f32x4{0.f, 0.f, 0.f, 0.f}; st.acc[1] = f32x4{0.f, 0.f, 0.f, 0.f};
    mainloop<N_FILL, 0>(st, S, L, wave, lane);
    *(LAS f32x4*)(L + L_OP + wave * 1024 + lane * 16) = st.acc[0];
    *(LAS f32x4*)(L + L_OP + 8192 + wave * 1024 + lane * 16) = st.acc[1];
    STAMP(4);
    wg_barrier();
    STAMP(5);
    if (wave == 0) combine(0, T);
    wg_barrier();
    STAMP(8);
    project(st.buf[80], st.buf[81], st.buf[82], st.buf[83], T);
    STAMP(9);
    if (w >= 72) { STAMP_OUT(); return; }
    v4u pa, pb;
    const int pvo = (tid >> 2) * 4608 + (tid & 3) * 16, pso = WS_PX + w * 64;
#pragma nounroll
    for (unsigned spins = 0; spins < SPIN_CAP; ++spins) {
      asm volatile("" ::: "memory");
      pa = ld128<AUX_SC1>(rws, pvo, pso); pb = ld128<AUX_SC1>(rws, pvo, pso + 128 * 4608);
      if (__all((bad4(pa, T) | bad4(pb, T)) == 0)) break;
    }
    STAMP(10);
    const f32x4 a = untag4(pa), c = untag4(pb);
    float e[4];
#pragma unroll
    for (int i = 0; i < 4; ++i) e[i] = t32_sum(a[i], c[i]);
    const float e0 = t16_sum(e[0], e[2]), e1 = t16_sum(e[1], e[3]);
    const bool b3 = lane & 8;
    float ev = (b3 ? e1 : e0) + dpp<ROR8>(b3 ? e0 : e1);
    ev += u2f((unsigned)__builtin_amdgcn_ds_swizzle((int)f2u(ev), 0x101F));
    if ((lane & 4) == 0) *(LAS float*)(L + L_RED + wave * 128 + ((lane >> 5) * 16 + (lane & 3) * 4 + ((lane >> 3) & 3)) * 4) = ev;
    wg_barrier();
    STAMP_OUT();
    return;
  }
  const unsigned T = ((unsigned)__builtin_amdgcn_readfirstlane(ep) + 1u) & 0xffu;
  qpartial();
  lds_wait_ge(flag, 9u);
  {
    f32x4 qs = *(LAS f32x4*)(L + L_QP + lane * 16);
#pragma unroll
    for (int i = 1; i < 9; ++i) qs += *(LAS f32x4*)(L + L_QP + i * 1024 + lane * 16);
    __builtin_amdgcn_raw_buffer_store_b128(tag4(qs, T), rws, dpos, WS_QX + (kvh * 16 + mi) * 1024, AUX_SC1);
    STAMP(2);
    v4u qx[8];
    const int qvo = (lane >> 5) * 4096 + (lane & 31) * 16, qso = WS_QX + kvh * 16384 + b * 512;
#pragma nounroll
    for (unsigned spins = 0; spins < SPIN_CAP; ++spins) {
      asm volatile("" ::: "memory");
      unsigned bad = 0;
#pragma unroll
      for (int i = 0; i < 8; ++i) qx[i] = ld128<AUX_SC1>(rws, qvo, qso + (i >> 2) * 8192 + (i & 3) * 1024);
#pragma unroll
      for (int i = 0; i < 8; ++i) bad |= bad4(qx[i], T);
      if (__all(bad == 0)) break;
      __builtin_amdgcn_s_sleep(2);
    }
#pragma unroll
    for (int br = 0; br < 2; ++br) {
      const f32x4 v = (untag4(qx[br * 4]) + untag4(qx[br * 4 + 1])) + (untag4(qx[br * 4 + 2]) + untag4(qx[br * 4 + 3]));
      *(LAS v2u*)(L + L_Q + br * 512 + lane * 8) = v2u{cvtpk(v[0], v[1]), cvtpk(v[2], v[3])};
    }
    LGKM0();
    if (lane == 0) flag[1] = 1u;
    STAMP(3);
  }
  float gt;
  {
    const rsrc_t rg = mk(gw, DM * 2);
    const int xso = w >= 36 ? DM * 2 : 0;
    float z = 0.f;
#pragma unroll
    for (int i = 0; i < 5; ++i) {
      const int o = (i * 64 + lane) * 4;
      const unsigned gg = __builtin_amdgcn_raw_buffer_load_b32(rg, o, 0, 0);
      const unsigned x0 = __builtin_amdgcn_raw_buffer_load_b32(rx, o, xso, 0);
      z += rbf(u2f(x0 << 16) * u2f(gg << 16)) + rbf(u2f(x0 & 0xffff0000u) * u2f(gg & 0xffff0000u));
    }
    z = x32_sum(z); z = x16_sum(z); z += dpp<ROR8>(z); z += dpp<ROR4>(z); z += dpp<XOR2>(z); z += dpp<XOR1>(z);
    z = rbf(z);
    gt = rbf(1.f / (1.f + __expf(-z)));
    gt = (lane & 16) ? rbf(1.f - gt) : gt;
    PINV(gt);
  }
  const v4u wo0 = issue<80>(S), wo1 = issue<81>(S), wo2 = issue<82>(S), wo3 = issue<83>(S);
  VMWAIT(0);
  STAMP(4);
  wg_barrier();
  STAMP(5);
  combine(1, T);
  {
    STAMP(6);
    v4u ox[2], mx[2];
    const int ovo = (lane >> 3) * 2048 + (lane & 7) * 16, oso = WS_OX + kvh * 16 * 2048 + b * 1024 + (s >> 2) * 512 + pq * 128;
    const int mvo = (lane >> 3) * 128, mso = WS_ML + kvh * 16 * 128 + b * 32 + (s >> 2) * 16;
#pragma nounroll
    for (unsigned spins = 0; spins < SPIN_CAP; ++spins) {
      asm volatile("" ::: "memory");
      ox[0] = ld128<AUX_SC1>(rws, ovo, oso); ox[1] = ld128<AUX_SC1>(rws, ovo, oso + 8 * 2048);
      mx[0] = ld128<AUX_SC1>(rws, mvo, mso); mx[1] = ld128<AUX_SC1>(rws, mvo, mso + 8 * 128);
      const unsigned bad = bad4(ox[0], T) | bad4(ox[1], T) | bad4(mx[0], T) | bad4(mx[1], T);
      if (__all(bad == 0)) break;
    }
    STAMP(7);
    f32x4 o[2]; float ls[2];
#pragma unroll
    for (int bb = 0; bb < 2; ++bb) {
      const float m = u2f(mx[bb][0] & 0xffffff00u), l = u2f(mx[bb][1] & 0xffffff00u);
      float M = fmaxf(m, dpp<ROR8>(m)); M = x16_max(M); M = x32_max(M);
      const float f = __builtin_amdgcn_exp2f((m - M) * 1.4426950408889634f);
      o[bb] = f * untag4(ox[bb]); ls[bb] = f * l;
    }
    const bool b3 = lane & 8;
    float e[4];
#pragma unroll
    for (int i = 0; i < 4; ++i) { const float keep = b3 ? o[1][i] : o[0][i], send = b3 ? o[0][i] : o[1][i]; e[i] = keep + dpp<ROR8>(send); }
    float lt; { const float keep = b3 ? ls[1] : ls[0], send = b3 ? ls[0] : ls[1]; lt = keep + dpp<ROR8>(send); }
    lt = x16_sum(lt); lt = x32_sum(lt);
    const float e0 = t16_sum(e[0], e[1]), e1 = t16_sum(e[2], e[3]);
    const float ev = t32_sum(e0, e1) / lt;
    const int r = 4 * (lane & 7) + g;
    *(LAS unsigned*)(L + L_OF + (((lane >> 3) & 1) * 32 + ((r >> 1) & 3) * 8 + (r >> 3) * 2 + (r & 1)) * 4) = cvtpk(ev, ev);
  }
  wg_barrier();
  STAMP(8);
  project(wo0, wo1, wo2, wo3, T);
  STAMP(9);
  if (w >= 72) { STAMP_OUT(); return; }
  wg_barrier();
  {
    float v = 0.f;
#pragma unroll
    for (int i = 0; i < 8; ++i) v += *(LAS float*)(L + L_RED + i * 128 + (lane & 31) * 4);
    const float r = x16_sum(rbf(gt * rbf(v)));
    if (lane < 16) out[w * 16 + lane] = (unsigned short)(cvtpk(r, r) & 0xffffu);
    if (w == 0 && lane == 0) __builtin_amdgcn_raw_buffer_store_b32(T, rws, 0, WS_EPOCH, AUX_SC1);
    STAMP(11);
    STAMP_OUT();
  }
}

extern "C" void kernel_launch(void* const* d_in, const int* in_sizes, int n_in, void* d_out, int out_size, void* d_ws, size_t ws_size,
                              hipStream_t stream) {
  typedef const unsigned short* P;
  fused_decode<<<256, NTHR, 0, stream>>>((P)d_in[0], (P)d_in[1], (P)d_in[2], (P)d_in[3], (P)d_in[4], (P)d_in[6], (P)d_in[7], (P)d_in[8],
                                         (P)d_in[9], (P)d_in[10], (unsigned short*)d_out, (unsigned char*)d_ws);
}
```

```cpp
#define DIAGW 0
#define DIAG (100ull * ((((unsigned long long)kB) >> 8) & 0xff))
#include <hip/hip_runtime.h>
#include <stdint.h>

#define W_INFL 3
#define N_FILL 20
#define STAMPS 0

#define GAS __attribute__((address_space(1)))
#define LAS __attribute__((address_space(3)))
typedef unsigned v4u __attribute__((ext_vector_type(4)));
typedef unsigned v2u __attribute__((ext_vector_type(2)));
typedef float f32x4 __attribute__((ext_vector_type(4)));
typedef short bf16x8 __attribute__((ext_vector_type(8)));
typedef __bf16 bf16x2 __attribute__((ext_vector_type(2)));
typedef __amdgpu_buffer_rsrc_t rsrc_t;

constexpr int DM = 576, SK = 8192, DH = 128, WIN = 2048;
constexpr int NTHR = 576;
constexpr int AUX_NT = 2, AUX_SC1 = 16;
constexpr unsigned SPIN_CAP = 1u << 16;
constexpr int WS_EPOCH = 0;
constexpr int WS_QX = 4096;
constexpr int WS_OX = WS_QX + 256 * 1024;
constexpr int WS_ML = WS_OX + 256 * 2048;
constexpr int WS_PX = WS_ML + 256 * 128;
constexpr int WS_END = WS_PX + 256 * 4608;
constexpr int WS_STAMP = 4 * 1024 * 1024;
constexpr int L_QP = 0;
constexpr int L_Q = L_QP + 9 * 1024;
constexpr int L_OP = L_Q + 1024;
constexpr int L_ML = L_OP + 16384;
constexpr int L_OF = L_ML + 256;
constexpr int L_RED = L_OF + 256;
constexpr int L_FLAG = L_RED + 1024;
constexpr int L_END = L_FLAG + 64;
constexpr int LDS_BYTES = 84 * 1024;
static_assert(L_END <= LDS_BYTES, "lds");

__device__ __forceinline__ rsrc_t mk(const void* p, int bytes) { return __builtin_amdgcn_make_buffer_rsrc((void*)p, 0, bytes, 0x00020000); }
template <int AUX> __device__ __forceinline__ v4u ld128(rsrc_t r, int vo, int so) { return __builtin_amdgcn_raw_buffer_load_b128(r, vo, so, AUX); }
__device__ __forceinline__ f32x4 mfma(v4u a, v4u b, f32x4 c) {
  return __builtin_amdgcn_mfma_f32_16x16x32_bf16(__builtin_bit_cast(bf16x8, a), __builtin_bit_cast(bf16x8, b), c, 0, 0, 0);
}
__device__ __forceinline__ float u2f(unsigned u) { return __builtin_bit_cast(float, u); }
__device__ __forceinline__ unsigned f2u(float f) { return __builtin_bit_cast(unsigned, f); }
__device__ __forceinline__ unsigned cvtpk(float lo, float hi) {
  bf16x2 r; r.x = (__bf16)lo; r.y = (__bf16)hi; return __builtin_bit_cast(unsigned, r);
}
__device__ __forceinline__ float rbf(float x) { return u2f(cvtpk(x, x) & 0xffff0000u); }
template <int CTRL> __device__ __forceinline__ unsigned dppu(unsigned x) { return (unsigned)__builtin_amdgcn_mov_dpp((int)x, CTRL, 0xf, 0xf, true); }
template <int CTRL> __device__ __forceinline__ float dpp(float x) { return u2f(dppu<CTRL>(f2u(x))); }
constexpr int XOR1 = 0xB1, XOR2 = 0x4E, XOR3 = 0x1B, ROR8 = 0x128, ROR4 = 0x124;
__device__ __forceinline__ float x16_max(float x) { auto s = __builtin_amdgcn_permlane16_swap(f2u(x), f2u(x), false, false); return fmaxf(u2f(s[0]), u2f(s[1])); }
__device__ __forceinline__ float x32_max(float x) { auto s = __builtin_amdgcn_permlane32_swap(f2u(x), f2u(x), false, false); return fmaxf(u2f(s[0]), u2f(s[1])); }
__device__ __forceinline__ float x16_sum(float x) { auto s = __builtin_amdgcn_permlane16_swap(f2u(x), f2u(x), false, false); return u2f(s[0]) + u2f(s[1]); }
__device__ __forceinline__ float x32_sum(float x) { auto s = __builtin_amdgcn_permlane32_swap(f2u(x), f2u(x), false, false); return u2f(s[0]) + u2f(s[1]); }
__device__ __forceinline__ float t16_sum(float a, float b) { auto s = __builtin_amdgcn_permlane16_swap(f2u(a), f2u(b), false, false); return u2f(s[0]) + u2f(s[1]); }
__device__ __forceinline__ float t32_sum(float a, float b) { auto s = __builtin_amdgcn_permlane32_swap(f2u(a), f2u(b), false, false); return u2f(s[0]) + u2f(s[1]); }

__device__ __forceinline__ v4u tag4(f32x4 v, unsigned T) {
  return v4u{(f2u(v[0]) & 0xffffff00u) | T, (f2u(v[1]) & 0xffffff00u) | T, (f2u(v[2]) & 0xffffff00u) | T, (f2u(v[3]) & 0xffffff00u) | T};
}
__device__ __forceinline__ unsigned bad4(v4u d, unsigned T) { return ((d[0] ^ T) | (d[1] ^ T) | (d[2] ^ T) | (d[3] ^ T)) & 0xffu; }
__device__ __forceinline__ f32x4 untag4(v4u d) {
  return f32x4{u2f(d[0] & 0xffffff00u), u2f(d[1] & 0xffffff00u), u2f(d[2] & 0xffffff00u), u2f(d[3] & 0xffffff00u)};
}
#define VMWAIT(n) __builtin_amdgcn_s_waitcnt(((n) & 15) | (((n) >> 4) << 14) | 0x0F70)
#define LGKM0() asm volatile("s_waitcnt lgkmcnt(0)" ::: "memory")
#define SB() __builtin_amdgcn_sched_barrier(0)
#define PINV(x) asm volatile("" : "+v"(x))
#define PINS(x) asm volatile("" : "+s"(x))
constexpr int OOB = 0x40000000;
__device__ __forceinline__ void wg_barrier() { LGKM0(); __builtin_amdgcn_s_barrier(); asm volatile("" ::: "memory"); }
__device__ __forceinline__ void lds_wait_ge(volatile LAS unsigned* p, unsigned want) {
#pragma nounroll
  for (unsigned spins = 0; spins < (SPIN_CAP << 4); ++spins) {
    if ((unsigned)__builtin_amdgcn_readfirstlane(*p) >= want) break;
    __builtin_amdgcn_s_sleep(1);
  }
  asm volatile("" ::: "memory");
}
#define STAMP(i) do { SB(); tsv[i] = __builtin_amdgcn_s_memrealtime(); SB(); } while (0)
#define STAMP_OUT() do {} while (0)

template <int A> __device__ __forceinline__ v4u afrag(unsigned pp, const v4u& msk) {
  const unsigned v = dppu<A * 0x55>(pp);
  return v4u{v & msk[0], v & msk[1], v & msk[2], v & msk[3]};
}

struct Stream {
  rsrc_t kA, kB, vA, vB, wo;
  int kvo, vvo, ovo;
};
template <int K> __device__ __forceinline__ v4u issue(const Stream& S) {
  if constexpr (K < 8) return ld128<AUX_NT>(S.kA, S.kvo, (K >> 2) * 32768 + (K & 3) * 1024);
  else if constexpr (K < 40) return ld128<AUX_NT>(S.kB, S.kvo, ((K - 8) >> 2) * 32768 + (K & 3) * 1024);
  else if constexpr (K < 48) return ld128<AUX_NT>(S.vA, S.vvo, ((K - 40) >> 2) * 32768 + (K & 3) * 1024);
  else if constexpr (K < 80) return ld128<AUX_NT>(S.vB, S.vvo, ((K - 48) >> 2) * 32768 + (K & 3) * 1024);
  else return ld128<AUX_NT>(S.wo, S.ovo, (K - 80) * 8 * 1152);
}

struct State {
  v4u buf[84];
  v4u q[2];
  v4u msk;
  f32x4 d[4];
  float sc[10];
  unsigned pp[10];
  f32x4 acc[2];
  bool p1, p2;
};

template <int LO, int HI> __device__ __forceinline__ void softmax(State& st, LAS unsigned char* L, int br, int wave, int lane) {
  float m = st.sc[LO];
#pragma unroll
  for (int t = LO + 1; t < HI; ++t) m = fmaxf(m, st.sc[t]);
  m = fmaxf(m, dpp<XOR1>(m)); m = fmaxf(m, dpp<XOR2>(m)); m = x16_max(m); m = x32_max(m);
  float l = 0.f;
#pragma unroll
  for (int t = LO; t < HI; ++t) {
    const float p = __builtin_amdgcn_exp2f((st.sc[t] - m) * 1.4426950408889634f);
    l += p;
    st.pp[t] = cvtpk(p, p);
  }
  l += dpp<XOR1>(l); l += dpp<XOR2>(l); l = x16_sum(l); l = x32_sum(l);
  if ((lane & 0x37) == 0) *(LAS v2u*)(L + L_ML + ((br * 2 + (lane >> 3)) * 8 + wave) * 8) = v2u{f2u(m), f2u(l)};
}

template <int K> __device__ __forceinline__ void process(State& st, LAS unsigned char* L, int wave, int lane) {
  if constexpr (K < 40) {
    constexpr int a = K & 3, t = K >> 2, br = K < 8 ? 0 : 1;
    st.d[a] = mfma(st.buf[K], st.q[br], f32x4{0.f, 0.f, 0.f, 0.f});
    if constexpr (a == 3) {
      float w[4];
#pragma unroll
      for (int x = 0; x < 4; ++x) {
        const float lo = st.p1 ? st.d[1 ^ x][1] : st.d[0 ^ x][0];
        const float hi = st.p1 ? st.d[3 ^ x][3] : st.d[2 ^ x][2];
        w[x] = st.p2 ? hi : lo;
      }
      const float r = (w[0] + dpp<XOR1>(w[1])) + (dpp<XOR2>(w[2]) + dpp<XOR3>(w[3]));
      st.sc[t] = rbf(rbf(r) * 0.08838834764831845f);
      if constexpr (K == 7) softmax<0, 2>(st, L, 0, wave, lane);
      if constexpr (K == 39) softmax<2, 10>(st, L, 1, wave, lane);
    }
  } else {
    constexpr int a = K & 3, t = (K - 40) >> 2, br = K < 48 ? 0 : 1;
    st.acc[br] = mfma(afrag<a>(st.pp[t], st.msk), st.buf[K], st.acc[br]);
  }
}

template <int K, int HI> __device__ __forceinline__ void fill(State& st, const Stream& S) {
  if constexpr (K < HI) {
    VMWAIT(W_INFL - 1);
    st.buf[K] = issue<K>(S);
    SB();
    fill<K + 1, HI>(st, S);
  }
}
template <int P, int HI> __device__ __forceinline__ void proc_range(State& st, LAS unsigned char* L, int wave, int lane) {
  if constexpr (P < HI) {
    process<P>(st, L, wave, lane);
    proc_range<P + 1, HI>(st, L, wave, lane);
  }
}
constexpr int cmin(int a, int b) { return a < b ? a : b; }
template <int I, int P> __device__ __forceinline__ void mainloop(State& st, const Stream& S, LAS unsigned char* L, int wave, int lane) {
  if constexpr (I < 84) {
    VMWAIT(W_INFL - 1);
    st.buf[I] = issue<I>(S);
    SB();
    constexpr int PH = cmin(cmin(P + 4, I + 1 - W_INFL), 80);
    proc_range<P, PH>(st, L, wave, lane);
    SB();
    mainloop<I + 1, PH>(st, S, L, wave, lane);
  } else if constexpr (P < 80) {
    process<P>(st, L, wave, lane);
    SB();
    mainloop<I, P + 1>(st, S, L, wave, lane);
  }
}

__global__ void __launch_bounds__(NTHR) fused_decode(const unsigned short* __restrict__ x, const unsigned short* __restrict__ kA,
    const unsigned short* __restrict__ vA, const unsigned short* __restrict__ wqA, const unsigned short* __restrict__ woA,
    const unsigned short* __restrict__ kB, const unsigned short* __restrict__ vB, const unsigned short* __restrict__ wqB,
    const unsigned short* __restrict__ woB, const unsigned short* __restrict__ gw, unsigned short* __restrict__ out, unsigned char* ws) {
  __shared__ __attribute__((aligned(16))) unsigned char smem[LDS_BYTES];
  LAS unsigned char* L = (LAS unsigned char*)smem;
  volatile LAS unsigned* flag = (volatile LAS unsigned*)(L + L_FLAG);
  const int w = blockIdx.x, pair = w >> 3, s = w & 7, b = pair >> 4, kvh = pair & 15, mi = b * 8 + s;
  const int tid = threadIdx.x, lane = tid & 63, wave = __builtin_amdgcn_readfirstlane(tid >> 6);
  const int g = lane >> 4, n = lane & 15, j0 = n & 7;
  unsigned long long tsv[12] = {0, 0, 0, 0, 0, 0, 0, 0, 0, 0, 0, 0};
  STAMP(0);
  const rsrc_t rws = mk(ws, WS_END);
  const int phead = kvh * 2 + (s >> 2), pq = s & 3;
  const rsrc_t rwq = mk((b ? wqB : wqA) + (size_t)(pq * 144) * 4096 + phead * DH, 144 * 8192);
  const rsrc_t rx = mk(x, 2 * DM * 2);
  const unsigned ep = __builtin_amdgcn_raw_buffer_load_b32(rws, 0, WS_EPOCH, AUX_SC1);
  const v2u xr = __builtin_amdgcn_raw_buffer_load_b64(rx, ((n >> 3) * DM + pq * 144 + wave * 16 + g * 4) * 2, 0, 0);
  v4u wq[4];
#pragma unroll
  for (int i = 0; i < 4; ++i) wq[i] = ld128<AUX_NT>(rwq, (wave * 16 + g * 4) * 8192 + n * 16, i * 8192);
  SB();
  if (tid < 4) flag[tid] = 0u;
  State st;
#pragma unroll
  for (int i = 0; i < 4; ++i) st.msk[i] = ((j0 >> 1) == i) ? ((j0 & 1) ? 0xffff0000u : 0x0000ffffu) : 0u;
  st.p1 = lane & 1; st.p2 = lane & 2;
  Stream S;
  {
    const size_t base = (size_t)pair * SK * DH;
    const size_t offA = base + (size_t)(SK - WIN + s * 256) * DH, offB = base + (size_t)(s * 1024) * DH;
    S.kA = mk(kA + offA, 65536); S.vA = mk(vA + offA, 65536);
    S.kB = mk(kB + offB, 262144); S.vB = mk(vB + offB, 262144);
    S.wo = mk((b ? woB : woA) + (size_t)(phead * DH + pq * 32) * DM, 32 * 1152);
    S.kvo = wave * 4096 + (n >> 2) * 256 + g * 64 + (n & 3) * 16;
    S.vvo = wave * 4096 + lane * 16;
    S.ovo = (g * 2 + (n >> 3)) * 1152 + wave * 128 + j0 * 16;
  }
  wg_barrier();
  auto qpartial = [&]() {
    f32x4 qd = {0.f, 0.f, 0.f, 0.f};
#pragma unroll
    for (int i = 0; i < 4; ++i) {
      const unsigned xx = __builtin_amdgcn_perm(xr[i >> 1], xr[i >> 1], (i & 1) ? 0x03020302u : 0x01000100u);
      qd = mfma(v4u{xx & st.msk[0], xx & st.msk[1], xx & st.msk[2], xx & st.msk[3]}, wq[i], qd);
    }
    *(LAS f32x4*)(L + L_QP + wave * 1024 + lane * 16) = qd;
    LGKM0();
    if (lane == 0) __hip_atomic_fetch_add((LAS unsigned*)(L + L_FLAG), 1u, __ATOMIC_RELAXED, __HIP_MEMORY_SCOPE_WORKGROUP);
    STAMP(1);
    SB();
  };
  const int dpos = (g >> 1) * 512 + n * 32 + (g & 1) * 16;
  auto combine = [&](const int br, const unsigned T) {
    const int h = g >> 1;
    f32x4 ml[4];
#pragma unroll
    for (int i = 0; i < 4; ++i) ml[i] = *(LAS f32x4*)(L + L_ML + (br * 2 + h) * 64 + i * 16);
    f32x4 op[8];
#pragma unroll
    for (int i = 0; i < 8; ++i) op[i] = *(LAS f32x4*)(L + L_OP + br * 8192 + i * 1024 + lane * 16);
    float M = fmaxf(ml[0][0], ml[0][2]);
#pragma unroll
    for (int i = 1; i < 4; ++i) M = fmaxf(M, fmaxf(ml[i][0], ml[i][2]));
    float Ls = 0.f; f32x4 O = {0.f, 0.f, 0.f, 0.f};
#pragma unroll
    for (int i = 0; i < 8; ++i) {
      const float f = __builtin_amdgcn_exp2f((ml[i >> 1][(i & 1) * 2] - M) * 1.4426950408889634f);
      Ls = fmaf(f, ml[i >> 1][(i & 1) * 2 + 1], Ls);
      O += f * op[i];
    }
    __builtin_amdgcn_raw_buffer_store_b128(tag4(O, T), rws, dpos, WS_OX + (kvh * 16 + mi) * 2048 + br * 1024, AUX_SC1);
    __builtin_amdgcn_raw_buffer_store_b128(tag4(f32x4{M, Ls, 0.f, 0.f}, T), rws, (lane & 31) ? OOB : h * 16, WS_ML + (kvh * 16 + mi) * 128 + br * 32, AUX_SC1);
  };
  auto project = [&](const v4u wo0, const v4u wo1, const v4u wo2, const v4u wo3, const unsigned T) {
    const v4u f0 = *(LAS v4u*)(L + L_OF + ((n >> 3) * 32 + g * 8) * 4), f1 = *(LAS v4u*)(L + L_OF + ((n >> 3) * 32 + g * 8) * 4 + 16);
    const v4u msk = st.msk;
    f32x4 d0 = {0.f, 0.f, 0.f, 0.f}, d1 = {0.f, 0.f, 0.f, 0.f};
#define PROJ(WV, A0, A1) d0 = mfma(v4u{A0 & msk[0], A0 & msk[1], A0 & msk[2], A0 & msk[3]}, WV, d0); \
                         d1 = mfma(v4u{A1 & msk[0], A1 & msk[1], A1 & msk[2], A1 & msk[3]}, WV, d1);
    PROJ(wo0, f0[0], f0[1]) PROJ(wo1, f0[2], f0[3]) PROJ(wo2, f1[0], f1[1]) PROJ(wo3, f1[2], f1[3])
#undef PROJ
    f32x4 y;
#pragma unroll
    for (int i = 0; i < 4; ++i) y[i] = d0[i] + dpp<ROR8>(d1[i]);
    __builtin_amdgcn_raw_buffer_store_b128(tag4(y, T), rws, n < 8 ? (g >> 1) * 2304 + wave * 256 + n * 32 + (g & 1) * 16 : OOB, WS_PX + w * 4608, AUX_SC1);
  };

  if (wave < 8) {
    fill<0, W_INFL>(st, S);
    const unsigned T = ((unsigned)__builtin_amdgcn_readfirstlane(ep) + 1u) & 0xffu;
    qpartial();
    fill<W_INFL, N_FILL>(st, S);
    STAMP(2);
    lds_wait_ge(flag + 1, 1u);
    STAMP(3);
    st.q[0] = *(LAS v4u*)(L + L_Q + (n >> 3) * 256 + (g * 4 + (n & 3)) * 16);
    st.q[1] = *(LAS v4u*)(L + L_Q + 512 + (n >> 3) * 256 + (g * 4 + (n & 3)) * 16);
    st.acc[0] = f32x4{0.f, 0.f, 0.f, 0.f}; st.acc[1] = f32x4{0.f, 0.f, 0.f, 0.f};
    mainloop<N_FILL, 0>(st, S, L, wave, lane);
    *(LAS f32x4*)(L + L_OP + wave * 1024 + lane * 16) = st.acc[0];
    *(LAS f32x4*)(L + L_OP + 8192 + wave * 1024 + lane * 16) = st.acc[1];
    STAMP(4);
    wg_barrier();
    STAMP(5);
    if (wave == 0) combine(0, T);
    wg_barrier();
    STAMP(8);
    project(st.buf[80], st.buf[81], st.buf[82], st.buf[83], T);
    STAMP(9);
    if (w >= 72) { STAMP_OUT(); return; }
    v4u pa, pb;
    const int pvo = (tid >> 2) * 4608 + (tid & 3) * 16, pso = WS_PX + w * 64;
#pragma nounroll
    for (unsigned spins = 0; spins < SPIN_CAP; ++spins) {
      asm volatile("" ::: "memory");
      pa = ld128<AUX_SC1>(rws, pvo, pso); pb = ld128<AUX_SC1>(rws, pvo, pso + 128 * 4608);
      if (__all((bad4(pa, T) | bad4(pb, T)) == 0)) break;
    }
    STAMP(10);
    const f32x4 a = untag4(pa), c = untag4(pb);
    float e[4];
#pragma unroll
    for (int i = 0; i < 4; ++i) e[i] = t32_sum(a[i], c[i]);
    const float e0 = t16_sum(e[0], e[2]), e1 = t16_sum(e[1], e[3]);
    const bool b3 = lane & 8;
    float ev = (b3 ? e1 : e0) + dpp<ROR8>(b3 ? e0 : e1);
    ev += u2f((unsigned)__builtin_amdgcn_ds_swizzle((int)f2u(ev), 0x101F));
    if ((lane & 4) == 0) *(LAS float*)(L + L_RED + wave * 128 + ((lane >> 5) * 16 + (lane & 3) * 4 + ((lane >> 3) & 3)) * 4) = ev;
    wg_barrier();
    STAMP_OUT();
    return;
  }
  const unsigned T = ((unsigned)__builtin_amdgcn_readfirstlane(ep) + 1u) & 0xffu;
  qpartial();
  lds_wait_ge(flag, 9u);
  {
    f32x4 qs = *(LAS f32x4*)(L + L_QP + lane * 16);
#pragma unroll
    for (int i = 1; i < 9; ++i) qs += *(LAS f32x4*)(L + L_QP + i * 1024 + lane * 16);
    __builtin_amdgcn_raw_buffer_store_b128(tag4(qs, T), rws, dpos, WS_QX + (kvh * 16 + mi) * 1024, AUX_SC1);
    STAMP(2);
    v4u qx[8];
    const int qvo = (lane >> 5) * 4096 + (lane & 31) * 16, qso = WS_QX + kvh * 16384 + b * 512;
#pragma nounroll
    for (unsigned spins = 0; spins < SPIN_CAP; ++spins) {
      asm volatile("" ::: "memory");
      unsigned bad = 0;
#pragma unroll
      for (int i = 0; i < 8; ++i) qx[i] = ld128<AUX_SC1>(rws, qvo, qso + (i >> 2) * 8192 + (i & 3) * 1024);
#pragma unroll
      for (int i = 0; i < 8; ++i) bad |= bad4(qx[i], T);
      if (__all(bad == 0)) break;
      __builtin_amdgcn_s_sleep(2);
    }
#pragma unroll
    for (int br = 0; br < 2; ++br) {
      const f32x4 v = (untag4(qx[br * 4]) + untag4(qx[br * 4 + 1])) + (untag4(qx[br * 4 + 2]) + untag4(qx[br * 4 + 3]));
      *(LAS v2u*)(L + L_Q + br * 512 + lane * 8) = v2u{cvtpk(v[0], v[1]), cvtpk(v[2], v[3])};
    }
    LGKM0();
    if (lane == 0) flag[1] = 1u;
    STAMP(3);
  }
  float gt;
  {
    const rsrc_t rg = mk(gw, DM * 2);
    const int xso = w >= 36 ? DM * 2 : 0;
    float z = 0.f;
#pragma unroll
    for (int i = 0; i < 5; ++i) {
      const int o = (i * 64 + lane) * 4;
      const unsigned gg = __builtin_amdgcn_raw_buffer_load_b32(rg, o, 0, 0);
      const unsigned x0 = __builtin_amdgcn_raw_buffer_load_b32(rx, o, xso, 0);
      z += rbf(u2f(x0 << 16) * u2f(gg << 16)) + rbf(u2f(x0 & 0xffff0000u) * u2f(gg & 0xffff0000u));
    }
    z = x32_sum(z); z = x16_sum(z); z += dpp<ROR8>(z); z += dpp<ROR4>(z); z += dpp<XOR2>(z); z += dpp<XOR1>(z);
    z = rbf(z);
    gt = rbf(1.f / (1.f + __expf(-z)));
    gt = (lane & 16) ? rbf(1.f - gt) : gt;
    PINV(gt);
  }
  const v4u wo0 = issue<80>(S), wo1 = issue<81>(S), wo2 = issue<82>(S), wo3 = issue<83>(S);
  VMWAIT(0);
  STAMP(4);
  wg_barrier();
  STAMP(5);
  combine(1, T);
  {
    STAMP(6);
    v4u ox[2], mx[2];
    const int ovo = (lane >> 3) * 2048 + (lane & 7) * 16, oso = WS_OX + kvh * 16 * 2048 + b * 1024 + (s >> 2) * 512 + pq * 128;
    const int mvo = (lane >> 3) * 128, mso = WS_ML + kvh * 16 * 128 + b * 32 + (s >> 2) * 16;
#pragma nounroll
    for (unsigned spins = 0; spins < SPIN_CAP; ++spins) {
      asm volatile("" ::: "memory");
      ox[0] = ld128<AUX_SC1>(rws, ovo, oso); ox[1] = ld128<AUX_SC1>(rws, ovo, oso + 8 * 2048);
      mx[0] = ld128<AUX_SC1>(rws, mvo, mso); mx[1] = ld128<AUX_SC1>(rws, mvo, mso + 8 * 128);
      const unsigned bad = bad4(ox[0], T) | bad4(ox[1], T) | bad4(mx[0], T) | bad4(mx[1], T);
      if (__all(bad == 0)) break;
    }
    STAMP(7);
    f32x4 o[2]; float ls[2];
#pragma unroll
    for (int bb = 0; bb < 2; ++bb) {
      const float m = u2f(mx[bb][0] & 0xffffff00u), l = u2f(mx[bb][1] & 0xffffff00u);
      float M = fmaxf(m, dpp<ROR8>(m)); M = x16_max(M); M = x32_max(M);
      const float f = __builtin_amdgcn_exp2f((m - M) * 1.4426950408889634f);
      o[bb] = f * untag4(ox[bb]); ls[bb] = f * l;
    }
    const bool b3 = lane & 8;
    float e[4];
#pragma unroll
    for (int i = 0; i < 4; ++i) { const float keep = b3 ? o[1][i] : o[0][i], send = b3 ? o[0][i] : o[1][i]; e[i] = keep + dpp<ROR8>(send); }
    float lt; { const float keep = b3 ? ls[1] : ls[0], send = b3 ? ls[0] : ls[1]; lt = keep + dpp<ROR8>(send); }
    lt = x16_sum(lt); lt = x32_sum(lt);
    const float e0 = t16_sum(e[0], e[1]), e1 = t16_sum(e[2], e[3]);
    const float ev = t32_sum(e0, e1) / lt;
    const int r = 4 * (lane & 7) + g;
    *(LAS unsigned*)(L + L_OF + (((lane >> 3) & 1) * 32 + ((r >> 1) & 3) * 8 + (r >> 3) * 2 + (r & 1)) * 4) = cvtpk(ev, ev);
  }
  wg_barrier();
  STAMP(8);
  project(wo0, wo1, wo2, wo3, T);
  STAMP(9);
  if (w >= 72) { STAMP_OUT(); return; }
  wg_barrier();
  {
    float v = 0.f;
#pragma unroll
    for (int i = 0; i < 8; ++i) v += *(LAS float*)(L + L_RED + i * 128 + (lane & 31) * 4);
    const float r = x16_sum(rbf(gt * rbf(v)));
    if (lane < 16) out[w * 16 + lane] = (unsigned short)(cvtpk(r, r) & 0xffffu);
    if (w == 0 && lane == 0) __builtin_amdgcn_raw_buffer_store_b32(T, rws, 0, WS_EPOCH, AUX_SC1);
    STAMP(11);
    STAMP_OUT();
    if (w == DIAGW) { const unsigned long long until = tsv[0] + 10000ull + (DIAG);
      for (unsigned sp = 0; sp < 10000000u; ++sp) { if (__builtin_amdgcn_s_memrealtime() >= until) break; __builtin_amdgcn_s_sleep(1); } }
  }
}

extern "C" void kernel_launch(void* const* d_in, const int* in_sizes, int n_in, void* d_out, int out_size, void* d_ws, size_t ws_size,
                              hipStream_t stream) {
  typedef const unsigned short* P;
  fused_decode<<<256, NTHR, 0, stream>>>((P)d_in[0], (P)d_in[1], (P)d_in[2], (P)d_in[3], (P)d_in[4], (P)d_in[6], (P)d_in[7], (P)d_in[8],
                                         (P)d_in[9], (P)d_in[10], (unsigned short*)d_out, (unsigned char*)d_ws);
}
```

```cpp
#include <hip/hip_runtime.h>
#include <stdint.h>

#define GAS __attribute__((address_space(1)))
#define LAS __attribute__((address_space(3)))
#define CAS __attribute__((address_space(4)))
typedef unsigned v4u __attribute__((ext_vector_type(4)));
typedef unsigned v2u __attribute__((ext_vector_type(2)));
typedef float f32x4 __attribute__((ext_vector_type(4)));
typedef float f32x2 __attribute__((ext_vector_type(2)));
typedef unsigned long long v8p __attribute__((ext_vector_type(8)));
typedef unsigned long long v4p __attribute__((ext_vector_type(4)));
typedef short bf16x8 __attribute__((ext_vector_type(8)));
typedef short v4s __attribute__((ext_vector_type(4)));
typedef __bf16 bf16x2 __attribute__((ext_vector_type(2)));
typedef __amdgpu_buffer_rsrc_t rsrc_t;

constexpr int DM = 576, SK = 8192, DH = 128, WIN = 2048;
constexpr int NTHR = 640;
constexpr int AUX_NT = 2, AUX_SC1 = 16;
constexpr unsigned SPIN_CAP = 1u << 16;
constexpr float LOG2E = 1.4426950408889634f;
constexpr int W_FAST = 4, W_NORM = 3, W_SLOW = 2;
constexpr int LEAD = 100;
constexpr int TAU = 29;
constexpr int PUB = 21;
constexpr int NB0 = 2;
constexpr int N_FILL = 24;
constexpr int PACE_END = 76;
constexpr int DLY_Q = 500;
constexpr int CODE_BYTES = 17920;
constexpr int WS_PACE = 4096;
constexpr int WS_FL = WS_PACE + 32768;
constexpr int WS_MB = WS_FL + 32768, MBS = 3200;
constexpr int WS_QX = WS_MB;
constexpr int WS_OA = WS_MB + 512;
constexpr int WS_OB = WS_MB + 1024;
constexpr int WS_PX = WS_MB + 2048;
constexpr int WS_ML = WS_MB + 3072;
constexpr int WS_END = WS_MB + 256 * MBS;
constexpr int L_FLAG = 0;
constexpr int L_PROG = 32, L_MODE = 64;
constexpr int L_ML = 128;
constexpr int L_F = L_ML + 256;
constexpr int L_W = L_F + 128;
constexpr int L_Q = L_W + 256;
constexpr int L_QP = L_Q + 1024;
constexpr int L_PP = L_QP + 10 * 512;
constexpr int L_OP = L_PP + 2 * 4608;
constexpr int L_WO = L_OP + 16384;
constexpr int L_END = L_WO + 9 * 4608;
constexpr int LDS_BYTES = 84 * 1024;
static_assert(L_END <= LDS_BYTES, "lds");

#define VMWAIT(n) __builtin_amdgcn_s_waitcnt(((n) & 15) | (((n) >> 4) << 14) | 0x0F70)
#define LGKM0() asm volatile("s_waitcnt lgkmcnt(0)" ::: "memory")
#define SB() __builtin_amdgcn_sched_barrier(0)
#define PINV(x) asm volatile("" : "+v"(x))
#define PINS(x) asm volatile("" : "+s"(x))
#define MEMFENCE() asm volatile("" ::: "memory")
#define ENDWAVE() do { __builtin_amdgcn_endpgm(); __builtin_unreachable(); } while (0)
#define INL __attribute__((always_inline))
constexpr int OOB = 0x40000000;

__device__ __forceinline__ rsrc_t mk(const void* p, int bytes) { return __builtin_amdgcn_make_buffer_rsrc((void*)p, 0, bytes, 0x00020000); }
template <int AUX> __device__ __forceinline__ v4u ld128(rsrc_t r, int vo, int so) { return __builtin_amdgcn_raw_buffer_load_b128(r, vo, so, AUX); }
__device__ __forceinline__ void st128(v4u v, rsrc_t r, int vo, int so) {
  __builtin_amdgcn_raw_buffer_store_b128(v, r, vo, so, AUX_SC1);
  SB(); asm volatile("s_nop 1"); SB();
}
__device__ __forceinline__ f32x4 mfma(v4u a, v4u b, f32x4 c) {
  return __builtin_amdgcn_mfma_f32_16x16x32_bf16(__builtin_bit_cast(bf16x8, a), __builtin_bit_cast(bf16x8, b), c, 0, 0, 0);
}
__device__ __forceinline__ v2u ldtr(LAS unsigned char* p) { return __builtin_bit_cast(v2u, __builtin_amdgcn_ds_read_tr16_b64_v4i16((LAS v4s*)p)); }
__device__ __forceinline__ float u2f(unsigned u) { return __builtin_bit_cast(float, u); }
__device__ __forceinline__ unsigned f2u(float f) { return __builtin_bit_cast(unsigned, f); }
__device__ __forceinline__ unsigned perm(unsigned hi, unsigned lo, unsigned sel) { return __builtin_amdgcn_perm(hi, lo, sel); }
__device__ __forceinline__ unsigned cvtpk(float lo, float hi) {
  bf16x2 r; r.x = (__bf16)lo; r.y = (__bf16)hi; return __builtin_bit_cast(unsigned, r);
}
__device__ __forceinline__ float rbf(float x) { return u2f(cvtpk(x, x) & 0xffff0000u); }
__device__ __forceinline__ float ex2(float x) { return __builtin_amdgcn_exp2f(x * LOG2E); }
template <int CTRL> __device__ __forceinline__ unsigned dppu(unsigned x) { return (unsigned)__builtin_amdgcn_mov_dpp((int)x, CTRL, 0xf, 0xf, true); }
template <int CTRL> __device__ __forceinline__ float dpp(float x) { return u2f(dppu<CTRL>(f2u(x))); }
constexpr int XOR1 = 0xB1, XOR2 = 0x4E, XOR3 = 0x1B, XOR7 = 0x141, ROR8 = 0x128, ROR4 = 0x124;
__device__ __forceinline__ float x16_max(float x) { auto s = __builtin_amdgcn_permlane16_swap(f2u(x), f2u(x), false, false); return fmaxf(u2f(s[0]), u2f(s[1])); }
__device__ __forceinline__ float x32_max(float x) { auto s = __builtin_amdgcn_permlane32_swap(f2u(x), f2u(x), false, false); return fmaxf(u2f(s[0]), u2f(s[1])); }
__device__ __forceinline__ float t16_sum(float a, float b) { auto s = __builtin_amdgcn_permlane16_swap(f2u(a), f2u(b), false, false); return u2f(s[0]) + u2f(s[1]); }
__device__ __forceinline__ float t32_sum(float a, float b) { auto s = __builtin_amdgcn_permlane32_swap(f2u(a), f2u(b), false, false); return u2f(s[0]) + u2f(s[1]); }
__device__ __forceinline__ float x16_sum(float x) { return t16_sum(x, x); }
__device__ __forceinline__ float x32_sum(float x) { return t32_sum(x, x); }
__device__ __forceinline__ v4u sparse(unsigned v, const v4u& msk) { return v4u{v & msk[0], v & msk[1], v & msk[2], v & msk[3]}; }
__device__ __forceinline__ unsigned tag8(float v, unsigned T) { return perm(f2u(v), T, 0x07060500u); }

__device__ __forceinline__ void wg_barrier() { LGKM0(); __builtin_amdgcn_s_barrier(); MEMFENCE(); }
__device__ __forceinline__ void lds_wait_ge(LAS unsigned char* L, int word, unsigned want) {
#pragma nounroll
  for (unsigned spins = 0; spins < (SPIN_CAP << 4); ++spins) {
    if ((unsigned)__builtin_amdgcn_readfirstlane(*(volatile LAS unsigned*)(L + L_FLAG + word * 4)) >= want) break;
    __builtin_amdgcn_s_sleep(1);
  }
  MEMFENCE();
}
template <int WORD> __device__ __forceinline__ void lds_count(LAS unsigned char* L) {
  asm volatile("s_mov_b64 exec, 1\n\tds_add_u32 %0, %1 offset:%2\n\ts_mov_b64 exec, -1" ::"v"((unsigned)(size_t)L), "v"(1u), "n"(L_FLAG + WORD * 4) : "memory");
}
__device__ __forceinline__ unsigned now32() { return (unsigned)__builtin_amdgcn_s_memrealtime(); }

struct Stream {
  rsrc_t ka, kb, va, vb, wo;
  int kvo, vvo, ovo;
};
constexpr int aoff(int k) { return ((k & 3) << 10) | ((k >> 2) << 17); }
constexpr int boff(int k) { return ((k & 3) << 10) | (((k >> 2) & 3) << 16) | ((k >> 4) << 20); }
template <int K> __device__ __forceinline__ v4u issue(const Stream& S) {
  if constexpr (K < 8) return ld128<AUX_NT>(S.ka, S.kvo, aoff(K));
  else if constexpr (K < 40) return ld128<AUX_NT>(S.kb, S.kvo, boff(K - 8));
  else if constexpr (K < 48) return ld128<AUX_NT>(S.va, S.vvo, aoff(K - 40));
  else if constexpr (K < 80) return ld128<AUX_NT>(S.vb, S.vvo, boff(K - 48));
  else return ld128<AUX_NT>(S.wo, S.ovo, (K - 80) * 8 * 1152);
}
constexpr int id_at(int pos) { return pos < 8 ? pos : pos < 16 ? pos + 32 : pos < 48 ? pos - 8 : pos; }

struct State {
  v4u buf[84];
  v4u q[2];
  v4u msk;
  f32x4 d[4];
  float sc[10];
  unsigned pp[10];
  f32x4 acc[2];
  bool p1, p2;
  unsigned mode, mnext;
};

template <int LO, int HI> __device__ __forceinline__ void softmax(State& st, LAS unsigned char* L, int br, int wave, int lane) {
  float m = st.sc[LO];
#pragma unroll
  for (int t = LO + 1; t < HI; ++t) m = fmaxf(m, st.sc[t]);
  m = fmaxf(m, dpp<XOR1>(m)); m = fmaxf(m, dpp<XOR2>(m)); m = x16_max(m); m = x32_max(m);
  float l = 0.f;
#pragma unroll
  for (int t = LO; t < HI; ++t) {
    const float p = ex2(st.sc[t] - m);
    l += p;
    st.pp[t] = cvtpk(p, p);
  }
  l += dpp<XOR1>(l); l += dpp<XOR2>(l); l = x16_sum(l); l = x32_sum(l);
  *(LAS v2u*)(L + L_ML + ((br * 2 + ((lane >> 3) & 1)) * 8 + wave) * 8) = v2u{f2u(m), f2u(l)};
}

template <int K> __device__ __forceinline__ void process(State& st, LAS unsigned char* L, int wave, int lane) {
  if constexpr (K < 40) {
    constexpr int a = K & 3, t = K >> 2, br = t < 2 ? 0 : 1;
    st.d[a] = mfma(st.buf[K], st.q[br], f32x4{0.f, 0.f, 0.f, 0.f});
    if constexpr (a == 3) {
      float w[4];
#pragma unroll
      for (int x = 0; x < 4; ++x) {
        const float lo = st.p1 ? st.d[1 ^ x][1] : st.d[0 ^ x][0];
        const float hi = st.p1 ? st.d[3 ^ x][3] : st.d[2 ^ x][2];
        w[x] = st.p2 ? hi : lo;
      }
      const float r = (w[0] + dpp<XOR1>(w[1])) + (dpp<XOR2>(w[2]) + dpp<XOR3>(w[3]));
      st.sc[t] = rbf(rbf(r) * 0.08838834764831845f);
      if constexpr (t == 1) softmax<0, 2>(st, L, 0, wave, lane);
      if constexpr (t == 9) { softmax<2, 10>(st, L, 1, wave, lane); lds_count<2>(L); }
    }
  } else {
    constexpr int a = K & 3, t = (K - 40) >> 2, br = t < 2 ? 0 : 1;
    if constexpr (K == 79) lds_count<5>(L);
    st.acc[br] = mfma(sparse(dppu<a * 0x55>(st.pp[t]), st.msk), st.buf[K], st.acc[br]);
    if constexpr (K == 48) { *(LAS f32x4*)(L + L_OP + wave * 1024 + lane * 16) = st.acc[0]; lds_count<3>(L); }
  }
}
template <int P, int HI> __device__ __forceinline__ void proc_range(State& st, LAS unsigned char* L, int wave, int lane) {
  if constexpr (P < HI) {
    process<id_at(P)>(st, L, wave, lane);
    proc_range<P + 1, HI>(st, L, wave, lane);
  }
}
template <int I> __device__ __forceinline__ void step(State& st, const Stream& S, LAS unsigned char* L, int wave) {
  asm volatile("s_cmp_lt_u32 %0, 2\n\ts_cbranch_scc1 1f\n\ts_waitcnt vmcnt(%2)\n1:\n\ts_cmp_lt_u32 %0, 1\n\ts_cbranch_scc1 2f\n\ts_waitcnt vmcnt(%1)\n2:"
               ::"s"(st.mode), "n"(W_NORM - 1), "n"(W_SLOW - 1) : "scc", "memory");
  VMWAIT(W_FAST - 1);
  st.buf[id_at(I)] = issue<id_at(I)>(S);
  SB();
  if constexpr ((I & 1) == 0 && I <= PACE_END) {
    st.mode = __builtin_amdgcn_readfirstlane(st.mnext);
    *(volatile LAS unsigned*)(L + L_PROG + wave * 4) = I;
    st.mnext = *(volatile LAS unsigned*)(L + L_MODE + wave * 4);
  }
}
template <int I, int HI> __device__ __forceinline__ void fill(State& st, const Stream& S, LAS unsigned char* L, int wave) {
  if constexpr (I < HI) {
    step<I>(st, S, L, wave);
    SB();
    fill<I + 1, HI>(st, S, L, wave);
  }
}
constexpr int cmin(int a, int b) { return a < b ? a : b; }
template <int I, int P> __device__ __forceinline__ void mainloop(State& st, const Stream& S, LAS unsigned char* L, int wave, int lane) {
  if constexpr (I < 80) {
    step<I>(st, S, L, wave);
    constexpr int PH = cmin(P + 4, I + 1 - W_FAST);
    proc_range<P, PH>(st, L, wave, lane);
    SB();
    mainloop<I + 1, PH>(st, S, L, wave, lane);
  } else if constexpr (P < 80) {
    proc_range<P, P + 1>(st, L, wave, lane);
    SB();
    mainloop<I, P + 1>(st, S, L, wave, lane);
  }
}

__global__ void __launch_bounds__(NTHR, 3) fused_decode(const void*, const void*, const void*, const void*, const void*, const void*,
                                                        const void*, const void*, const void*, const void*, void*, void*) {
  v8p pa; v4p pb;
  asm volatile("s_load_dwordx16 %0, %2, 0x0\n\ts_load_dwordx8 %1, %2, 0x40" : "=&s"(pa), "=&s"(pb) : "s"(__builtin_amdgcn_kernarg_segment_ptr()) : "memory");
  SB();
  __shared__ __attribute__((aligned(16))) unsigned char smem[LDS_BYTES];
  LAS unsigned char* L = (LAS unsigned char*)smem;
  const int tid = threadIdx.x, lane = tid & 63, wave = __builtin_amdgcn_readfirstlane(tid >> 6);
  unsigned cp0 = 0, cp1 = 0, cp2 = 0;
  if (wave == 0) {
    unsigned long long pc; asm volatile("s_getpc_b64 %0" : "=s"(pc));
    const rsrc_t rc = mk((const void*)(pc & ~127ull), CODE_BYTES);
    cp0 = __builtin_amdgcn_raw_buffer_load_b32(rc, lane * 128, 0, 0); cp1 = __builtin_amdgcn_raw_buffer_load_b32(rc, lane * 128, 8192, 0);
    cp2 = __builtin_amdgcn_raw_buffer_load_b32(rc, lane * 128, 16384, 0);
  }
  SB();
  const int bid = blockIdx.x, w = (bid & 7) * 32 + (bid >> 3);
  const int pair = w >> 3, s = w & 7, b = pair >> 4, kvh = pair & 15;
  const int g = lane >> 4, n = lane & 15, j0 = n & 7;
  const int u = 9 * w + wave, ubr = (u >> 7) & 1, urb = u & 127;
  unsigned offa = (pair * SK + SK - WIN) * (DH * 2) + ((s & 3) << 15) + ((s >> 2) << 18), offb = pair * SK * (DH * 2) + ((s & 1) << 15) + ((s >> 1) << 18);
  unsigned offw = urb * (32 * DM * 2) + (u >> 8) * 128, offq = (s & 1) * (288 * 8192) + kvh * 512 + (s >> 1) * 128;
  int qvo = (wave * 32 + g * 8 + (n >> 3) * 4) * 8192 + j0 * 16, xvo = ((n >> 3) * DM + (s & 1) * 288 + wave * 32 + g * 8) * 2;
  PINS(offa); PINS(offb); PINS(offw); PINS(offq); PINV(qvo); PINV(xvo);
  unsigned long long t64;
  SB();
  asm volatile("s_memrealtime %2\n\ts_waitcnt lgkmcnt(0)" : "+s"(pa), "+s"(pb), "=&s"(t64)::"memory");
  SB();
  const unsigned t_start = (unsigned)t64;
  typedef const char* P;
  const P wqA = (P)pa[0], wqB = (P)pa[1], x = (P)pa[2], kA = (P)pa[3], kB = (P)pa[4], woA = (P)pa[5], woB = (P)pa[6], vA = (P)pa[7];
  const P vB = (P)pb[0], gw = (P)pb[1], ws = (P)pb[3];
  unsigned e0;
  asm volatile("s_cmp_lg_u32 %2, 9\n\ts_cbranch_scc1 1f\n\ts_load_dword %0, %1, 0x0\n\ts_barrier\n1:" : "=&s"(e0) : "s"(ws), "s"(wave) : "scc", "memory");

  if (wave == 9) {
    asm volatile("s_waitcnt lgkmcnt(0)" : "+s"(e0)::"memory");
    const rsrc_t rws = mk(ws, WS_END);
    const unsigned epp = e0 + 1u, tg = epp & 0xffffu;
    volatile LAS unsigned* prog = (volatile LAS unsigned*)(L + L_PROG) + j0;
    volatile LAS unsigned* modes = (volatile LAS unsigned*)(L + L_MODE) + j0;
    int ref = 0; bool have = false;
    unsigned until = ((t_start >> 8) + 1) << 8;
#pragma nounroll
    for (unsigned spins = 0; spins < SPIN_CAP; ++spins) {
      const int p = *prog;
      if (__any(p >= PACE_END)) break;
      const unsigned now = now32();
      if (have) {
        const int lead = (short)(p * (2 * TAU) - 2 * now - ref);
        *modes = lead > LEAD ? 2u : (lead < -LEAD ? 0u : 1u);
      }
      if ((int)(now - until) >= 0) {
        const int slot = WS_PACE + ((until >> 8) & 31) * 1024;
        if (until & 128) {
          MEMFENCE();
          const unsigned o = __builtin_amdgcn_raw_buffer_load_b32(rws, j0 * 128, slot, AUX_SC1);
          if (__all((o & 0xffffu) == tg)) {
            const int b0 = __builtin_amdgcn_readfirstlane(o >> 16);
            int d = (short)((o >> 16) - b0);
            d += dppu<XOR1>(d); d += dppu<XOR2>(d); d += dppu<XOR7>(d);
            ref = b0 + (d >> 3); have = true;
          }
        } else if ((bid >> 3) == PUB) {
          int sum = p + dppu<XOR1>(p); sum += dppu<XOR2>(sum); sum += dppu<XOR7>(sum);
          __builtin_amdgcn_raw_buffer_store_b32((((sum * TAU - 8 * now) >> 2) << 16) | tg, rws, lane ? OOB : (bid & 7) * 128, slot, AUX_SC1);
        }
        until += 128;
      } else __builtin_amdgcn_s_sleep(4);
    }
    __builtin_amdgcn_s_setprio(3);
    const bool need = lane < 32 && (((9 * w + g * 8) >> 7) & 1);
    int fpo = need ? WS_FL + w * 128 + lane * 4 : OOB;
    const int u0 = (lane >> 1) * 256 + 128 + kvh * 8, wc = (u0 + (lane & 1) * 7) / 9;
    int fvo = (lane < 18 && (!(lane & 1) || wc != u0 / 9)) ? WS_FL + wc * 128 + (((9 * wc) >> 3) == (u0 >> 3) ? 0 : 64) + (b * 8 + s) * 4 : OOB;
    unsigned epv = epp, want = need ? epp : 0u;
    PINV(fpo); PINV(fvo); PINV(epv); PINV(want);
    lds_wait_ge(L, 5, 8u);
    __builtin_amdgcn_raw_buffer_store_b32(epv, rws, fvo, 0, AUX_SC1);
    wg_barrier();
    unsigned f0, f1, f2, f3, cnt = SPIN_CAP;
    const v4u rd = {(unsigned)(size_t)ws, (unsigned)((size_t)ws >> 32) & 0xffffu, (unsigned)WS_END, 0x00020000u};
#define POLL(r) "v_mov_b32 " r ", 0\n\tbuffer_atomic_add " r ", %5, %6, 0 offen sc0 sc1\n\t"
#define TEST(r) "s_waitcnt vmcnt(3)\n\tv_cmp_ne_u32 vcc, " r ", %7\n\ts_cbranch_vccz 2f\n\ts_sleep 2\n\t"
    asm volatile(POLL("%0") "s_sleep 3\n\t" POLL("%1") "s_sleep 3\n\t" POLL("%2") "s_sleep 3\n"
                 "1:\n\t" POLL("%3") TEST("%0") POLL("%0") TEST("%1") POLL("%1") TEST("%2") POLL("%2") TEST("%3")
                 "s_sub_u32 %4, %4, 1\n\ts_cmp_lg_u32 %4, 0\n\ts_cbranch_scc1 1b\n"
                 "2:\n\ts_barrier\n\ts_waitcnt vmcnt(0)"
                 : "=&v"(f0), "=&v"(f1), "=&v"(f2), "=&v"(f3), "+s"(cnt) : "v"(fpo), "s"(rd), "v"(want) : "vcc", "scc", "memory");
#undef POLL
#undef TEST
    ENDWAVE();
  }

  const rsrc_t rwq = mk((b ? wqB : wqA) + offq, 287 * 8192 + 128);
  v4u wq[4];
#pragma unroll
  for (int i = 0; i < 4; ++i) wq[i] = ld128<AUX_NT>(rwq, qvo, i * 8192);
  const v4u xr = ld128<0>(mk(x, 2 * DM * 2), xvo, 0);
  SB();
  const P kap = kA + offa, wop = (ubr ? woB : woA) + offw;
  constexpr int A_SPAN = aoff(7) + 7 * 4096 + 1024, B_SPAN = boff(31) + 7 * 4096 + 1024, WO_SPAN = 31 * 1152 + 128;
  Stream S;
  S.kvo = wave * 4096 + (n >> 2) * 256 + g * 64 + (n & 3) * 16;
  S.ovo = (lane >> 3) * 1152 + j0 * 16;
  State st;
  if (tid >= 512 && tid < 536) *(LAS unsigned*)(L + L_FLAG + lane * 4) = lane < 16 ? 0u : 1u;
  {
    const rsrc_t r0 = mk(wave < 8 ? kap : wop, wave < 8 ? A_SPAN : WO_SPAN);
    const int vo0 = wave < 8 ? S.kvo : S.ovo, st0 = wave < 8 ? 1024 : 8 * 1152;
    SB(); wg_barrier(); SB();
#pragma unroll
    for (int i = 0; i < NB0; ++i) st.buf[i] = ld128<AUX_NT>(r0, vo0, i * st0);
  }
  SB();
  const rsrc_t rws = mk(ws, WS_END);
  S.ka = mk(kap, A_SPAN); S.va = mk(vA + offa, A_SPAN);
  S.kb = mk(kB + offb, B_SPAN); S.vb = mk(vB + offb, B_SPAN);
  S.wo = mk(wop, WO_SPAN);
  S.vvo = wave * 4096 + lane * 16;
#pragma unroll
  for (int i = 0; i < 4; ++i) st.msk[i] = ((j0 >> 1) == i) ? ((j0 & 1) ? 0xffff0000u : 0x0000ffffu) : 0u;
  st.p1 = lane & 1; st.p2 = lane & 2;
  {
    f32x4 d0 = {0.f, 0.f, 0.f, 0.f}, d1 = {0.f, 0.f, 0.f, 0.f};
#pragma unroll
    for (int i = 0; i < 4; ++i) {
      const unsigned sel = (i & 1) ? 0x03020302u : 0x01000100u;
      d0 = mfma(sparse(perm(xr[i >> 1], xr[i >> 1], sel), st.msk), wq[i], d0);
      d1 = mfma(sparse(perm(xr[2 + (i >> 1)], xr[2 + (i >> 1)], sel), st.msk), wq[i], d1);
    }
    f32x4 y;
#pragma unroll
    for (int i = 0; i < 4; ++i) y[i] = d0[i] + dpp<ROR8>(d1[i]);
    *(LAS f32x4*)(L + L_QP + (n < 8 ? wave : 9) * 512 + (g >> 1) * 256 + j0 * 32 + (g & 1) * 16) = y;
    lds_count<0>(L);
    asm volatile("" ::"v"(cp0), "v"(cp1), "v"(cp2));
    SB();
  }
  unsigned ep, T, T16;
  auto wo_frags = [&](const v4u w0, const v4u w1, const v4u w2, const v4u w3, v4u (&bf)[4]) INL {
    LAS unsigned char* wr = L + L_WO + wave * 4608 + (lane >> 3) * 144 + j0 * 16;
    *(LAS v4u*)(wr) = w0; *(LAS v4u*)(wr + 8 * 144) = w1; *(LAS v4u*)(wr + 16 * 144) = w2; *(LAS v4u*)(wr + 24 * 144) = w3;
    MEMFENCE();
    LAS unsigned char* rd = L + L_WO + wave * 4608 + (g * 8 + (n >> 2)) * 144 + (n & 3) * 8;
#pragma unroll
    for (int t = 0; t < 4; ++t) {
      const v2u lo = ldtr(rd + t * 32), hi = ldtr(rd + 4 * 144 + t * 32);
      bf[t] = v4u{lo[0], lo[1], hi[0], hi[1]};
    }
    PINV(bf[0]); PINV(bf[1]); PINV(bf[2]); PINV(bf[3]);
    SB();
  };
  int ovo = ((urb >> 3) * 8 + (lane >> 3)) * MBS + (ubr ? WS_OB + (urb & 7) * 128 + j0 * 16 : WS_OA + (urb & 7) * 64 + (j0 >> 1) * 16);
  unsigned psel = ubr ? 0x07060302u : (j0 & 1) ? 0x07060504u : 0x03020100u, tmask = ubr ? 0xffffu : 0u;
  int awa = L_WO + wave * 4608 + (lane >> 3) * 80 + j0 * 8, ara = L_WO + wave * 4608 + n * 80 + g * 16;
  int wra = L_W + (((u >> 3) - ((9 * w) >> 3)) * 2 + ((urb >> 2) & 1)) * 64 + g * 16;
  const int cut = 128 * (((9 * w) >> 7) + 1) - 9 * w;
  int pwa = L_PP + (wave >= cut ? 4608 : 0) + wave * 512 + (lane >> 5) * 256 + (lane & 31) * 4;
  auto gather = [&](v4u& o0, v4u& o1) INL { o0 = ld128<AUX_SC1>(rws, ovo, 0); o1 = ld128<AUX_SC1>(rws, ovo, 128 * MBS); };
  auto project = [&](const v4u (&bf)[4], v4u o0, v4u o1) INL {
    const f32x4 wt = *(LAS f32x4*)(L + wra);
    SB();
    v4u a;
    auto take = [&]() INL -> bool {
      *(LAS v2u*)(L + awa) = v2u{perm(o0[2], o0[0], psel), perm(o0[3], o0[1], psel)};
      *(LAS v2u*)(L + awa + 640) = v2u{perm(o1[2], o1[0], psel), perm(o1[3], o1[1], psel)};
      MEMFENCE();
      a = *(LAS v4u*)(L + ara);
      SB();
      const unsigned b0 = __builtin_amdgcn_bitop3_b32(o0[0], o0[1], T16, 0x7e), b1 = __builtin_amdgcn_bitop3_b32(o0[2], o0[3], T16, 0x7e);
      const unsigned b2 = __builtin_amdgcn_bitop3_b32(o1[0], o1[1], T16, 0x7e), b3 = __builtin_amdgcn_bitop3_b32(o1[2], o1[3], T16, 0x7e);
      return __all(((b0 | b1 | b2 | b3) & tmask) == 0);
    };
    unsigned spins = SPIN_CAP;
    bool ok = take();
#pragma nounroll
    while (!ok) {
      __builtin_amdgcn_s_sleep(2);
      MEMFENCE();
      gather(o0, o1);
      ok = take() | (--spins == 0);
    }
    f32x4 d[4];
#pragma unroll
    for (int t = 0; t < 4; ++t) d[t] = mfma(a, bf[t], f32x4{0.f, 0.f, 0.f, 0.f});
    SB();
    float sm[4];
#pragma unroll
    for (int t = 0; t < 4; ++t) { const f32x2 h = f32x2{wt[0], wt[1]} * f32x2{d[t][0], d[t][1]} + f32x2{wt[2], wt[3]} * f32x2{d[t][2], d[t][3]}; sm[t] = h[0] + h[1]; }
    *(LAS float*)(L + pwa) = t16_sum(sm[0], sm[1]);
    *(LAS float*)(L + pwa + 128) = t16_sum(sm[2], sm[3]);
  };
  auto publish_p = [&](const int pra, const int pxo) INL {
    f32x2 p[9];
#pragma unroll
    for (int j = 0; j < 9; ++j) p[j] = *(LAS f32x2*)(L + pra + j * 512);
    const f32x2 y = ((p[0] + p[1]) + (p[2] + p[3])) + ((p[4] + p[5]) + (p[6] + p[7] + p[8]));
    __builtin_amdgcn_raw_buffer_store_b64(v2u{tag8(y[0], T), tag8(y[1], T)}, rws, pxo, 0, AUX_SC1);
  };

  if (__builtin_expect(wave < 8, 1)) {
    st.mode = 1; st.mnext = 1;
    fill<NB0, N_FILL>(st, S, L, wave);
    lds_wait_ge(L, 1, 1u);
    ep = __builtin_amdgcn_readfirstlane(*(LAS unsigned*)(L + L_FLAG + 16)); T = ep & 0xffu; T16 = ep & 0xffffu;
    st.q[0] = *(LAS v4u*)(L + L_Q + (n >> 3) * 256 + (g * 4 + (n & 3)) * 16);
    st.q[1] = *(LAS v4u*)(L + L_Q + 512 + (n >> 3) * 256 + (g * 4 + (n & 3)) * 16);
    st.acc[0] = f32x4{0.f, 0.f, 0.f, 0.f}; st.acc[1] = f32x4{0.f, 0.f, 0.f, 0.f};
    int pra = L_PP + 4608 + lane * 8, pxo = WS_PX + w * MBS + 512 + lane * 8;
    PINV(ovo); PINV(pwa); PINV(T16); PINV(awa); PINV(ara); PINV(wra); PINV(psel); PINV(tmask); PINV(pra); PINV(pxo);
    mainloop<N_FILL, 0>(st, S, L, wave, lane);
    *(LAS f32x4*)(L + L_OP + 8192 + wave * 1024 + lane * 16) = st.acc[1];
    SB();
    st.buf[80] = issue<80>(S); st.buf[81] = issue<81>(S); st.buf[82] = issue<82>(S); st.buf[83] = issue<83>(S);
    SB();
    wg_barrier();
    wg_barrier();
    v4u bf[4], o0, o1;
    gather(o0, o1);
    SB();
    wo_frags(st.buf[80], st.buf[81], st.buf[82], st.buf[83], bf);
    project(bf, o0, o1);
    wg_barrier();
    if (wave == 0 && cut < 9) publish_p(pra, pxo);
    return;
  }

  __builtin_amdgcn_s_setprio(3);
  static_assert(NB0 == 2, "the rest of the control wave's wo tile");
  st.buf[2] = issue<82>(S); st.buf[3] = issue<83>(S);
  ep = *(const CAS unsigned*)ws + 1u;
  PINS(ep);
  T = ep & 0xffu; T16 = ep & 0xffffu;
  *(LAS unsigned*)(L + L_FLAG + 16) = ep;
  v4u bf[4];
  lds_wait_ge(L, 0, 9u);
  {
    f32x4 qs = *(LAS f32x4*)(L + L_QP + (lane & 31) * 16);
#pragma unroll
    for (int i = 1; i < 9; ++i) qs += *(LAS f32x4*)(L + L_QP + i * 512 + (lane & 31) * 16);
    st128(v4u{tag8(qs[0], T), tag8(qs[1], T), tag8(qs[2], T), tag8(qs[3], T)}, rws, lane < 32 ? lane * 16 : OOB, WS_QX + w * MBS);
  }
  wo_frags(st.buf[0], st.buf[1], st.buf[2], st.buf[3], bf);
#pragma nounroll
  for (unsigned spins = 0; spins < 4096 && (int)(now32() - t_start) < DLY_Q; ++spins) __builtin_amdgcn_s_sleep(2);
  {
    v4u qx[4];
    const int gvo = g * 2 * MBS + n * 16, gso = WS_QX + kvh * 8 * MBS + b * 256;
#pragma nounroll
    for (unsigned spins = 0; spins < SPIN_CAP; ++spins) {
      MEMFENCE();
#pragma unroll
      for (int i = 0; i < 4; ++i) qx[i] = ld128<AUX_SC1>(rws, gvo, gso + ((i >> 1) * 128 + (i & 1)) * MBS) ^ T;
      const v4u bad = qx[0] | qx[1] | qx[2] | qx[3];
      if (__all(((bad[0] | bad[1] | bad[2] | bad[3]) & 0xffu) == 0)) break;
      __builtin_amdgcn_s_sleep(16);
    }
#pragma unroll
    for (int br = 0; br < 2; ++br) {
      const f32x4 v = __builtin_bit_cast(f32x4, qx[br * 2]) + __builtin_bit_cast(f32x4, qx[br * 2 + 1]);
      *(LAS v2u*)(L + L_Q + br * 512 + lane * 8) = v2u{cvtpk(v[0], v[1]), cvtpk(v[2], v[3])};
    }
    lds_count<1>(L);
  }
  float gt = 0.f;
  int rvo[4] = {OOB, OOB, OOB, OOB};
  if (w < 72) {
    const rsrc_t rg = mk(gw, DM * 2), rxr = mk(x + ((w >> 2) & 1) * (DM * 2), DM * 2);
    float z = 0.f;
#pragma unroll
    for (int i = 0; i < 5; ++i) {
      const unsigned gg = __builtin_amdgcn_raw_buffer_load_b32(rg, (i * 64 + lane) * 4, 0, 0), xx = __builtin_amdgcn_raw_buffer_load_b32(rxr, (i * 64 + lane) * 4, 0, 0);
      z += rbf(u2f(xx << 16) * u2f(gg << 16)) + rbf(u2f(xx & 0xffff0000u) * u2f(gg & 0xffff0000u));
    }
    z = x32_sum(z); z = x16_sum(z); z += dpp<ROR8>(z); z += dpp<ROR4>(z); z += dpp<XOR2>(z); z += dpp<XOR1>(z);
    gt = rbf(1.f / (1.f + __expf(-rbf(z))));
    gt = (lane & 32) ? rbf(1.f - gt) : gt;
#pragma unroll
    for (int i = 0; i < 4; ++i) {
      const int cs = (w >> 3) * 2 + (i >> 1), wp = (128 * cs) / 9 + (i & 1) * 8 + (lane >> 3);
      rvo[i] = 9 * wp <= 128 * cs + 127 ? WS_PX + wp * MBS + (((9 * wp) >> 7) == cs ? 0 : 512) + (w & 7) * 64 + j0 * 8 : OOB;
    }
  }
  unsigned tl[4];
#pragma unroll
  for (int i = 0; i < 4; ++i) { tl[i] = rvo[i] == OOB ? 0u : T; PINV(tl[i]); PINV(rvo[i]); }
  PINV(gt);
  lds_wait_ge(L, 2, 8u);
  v2u mlw;
  {
    const v2u ml = *(LAS v2u*)(L + L_ML + (lane & 31) * 8);
    const float m = u2f(ml[0]);
    float M = fmaxf(m, dpp<XOR1>(m)); M = fmaxf(M, dpp<XOR2>(M)); M = fmaxf(M, dpp<XOR7>(M));
    const float e = ex2(m - M);
    float Ls = e * u2f(ml[1]);
    PINV(Ls);
    Ls += dpp<XOR1>(Ls); Ls += dpp<XOR2>(Ls); Ls += dpp<XOR7>(Ls);
    Ls = u2f(f2u(Ls) & 0xffffff00u);
    *(LAS float*)(L + L_F + (lane & 31) * 4) = e / Ls;
    mlw = v2u{(f2u(M) & 0xffff0000u) | T16, f2u(Ls) | T};
    MEMFENCE();
  }
  auto combine = [&](const int br, const f32x4 f0, const f32x4 f1) INL -> f32x4 {
    f32x4 op[8];
#pragma unroll
    for (int i = 0; i < 8; ++i) op[i] = *(LAS f32x4*)(L + L_OP + br * 8192 + i * 1024 + lane * 16);
    f32x4 O = f0[0] * op[0];
    O += f0[1] * op[1]; O += f0[2] * op[2]; O += f0[3] * op[3];
    O += f1[0] * op[4]; O += f1[1] * op[5]; O += f1[2] * op[6]; O += f1[3] * op[7];
    return O;
  };
  const int dpos = (g >> 1) * 128 + n * 8 + (g & 1) * 4, fa = L_F + (g >> 1) * 32;
  lds_wait_ge(L, 3, 8u);
  {
    const f32x4 O = combine(0, *(LAS f32x4*)(L + fa), *(LAS f32x4*)(L + fa + 16));
    __builtin_amdgcn_raw_buffer_store_b64(v2u{cvtpk(O[0], O[1]), cvtpk(O[2], O[3])}, rws, dpos * 2, WS_OA + w * MBS, AUX_SC1);
    asm volatile("s_waitcnt vmcnt(0)" ::: "memory");
    __builtin_amdgcn_raw_buffer_store_b64(mlw, rws, (lane & 0x27) ? OOB : lane, WS_ML + w * MBS, AUX_SC1);
  }
  f32x4 fb0 = *(LAS f32x4*)(L + fa + 64), fb1 = *(LAS f32x4*)(L + fa + 80);
  if (cut < 9) {
#pragma unroll
    for (int j = 0; j < 9; ++j) *(LAS v4u*)(L + L_PP + j * 1024 + lane * 16) = v4u{0u, 0u, 0u, 0u};
  }
  const int uu = 9 * w + (lane >> 5) * 8;
  int svo = WS_ML + ((g & 1) * 128 + ((uu & 127) >> 3) * 8 + ((lane >> 1) & 7)) * MBS + ((uu >> 7) & 1) * 16 + (lane & 1) * 8;
  int wwa = L_W + (((lane >> 5) * 2 + (lane & 1)) * 16 + ((lane >> 1) & 15)) * 4;
  int obo = WS_OB + w * MBS + dpos * 4, pxo = WS_PX + w * MBS + lane * 8, pra = L_PP + lane * 8;
  GAS unsigned short* op = (GAS unsigned short*)pb[2] + ((w >> 2) & 1) * DM + (w >> 3) * 64 + (w & 3) * 16 + j0 * 2 + (g & 1);
  unsigned sl = 0x05040100u, sh = 0x07060100u;
  PINV(awa); PINV(ara); PINV(wra); PINV(psel); PINV(tmask); PINV(ovo); PINV(pwa); PINV(T16);
  PINV(fb0); PINV(fb1); PINV(obo); PINV(pxo); PINV(pra); PINV(op); PINV(svo); PINV(wwa); PINV(sl); PINV(sh);
  VMWAIT(0);
  wg_barrier();
  v2u sx = __builtin_amdgcn_raw_buffer_load_b64(rws, svo, 0, AUX_SC1);
  SB();
  {
    const f32x4 O = combine(1, fb0, fb1);
    const unsigned c0 = cvtpk(O[0], O[1]), c1 = cvtpk(O[2], O[3]);
    st128(v4u{perm(c0, T16, sl), perm(c1, T16, sl), perm(c0, T16, sh), perm(c1, T16, sh)}, rws, obo, 0);
  }
  {
    sx ^= v2u{T16, T};
    unsigned spins = SPIN_CAP;
    bool ok = __all(((sx[0] & 0xffffu) | (sx[1] & 0xffu)) == 0);
#pragma nounroll
    while (!ok) {
      __builtin_amdgcn_s_sleep(8);
      MEMFENCE();
      sx = __builtin_amdgcn_raw_buffer_load_b64(rws, svo, 0, AUX_SC1) ^ v2u{T16, T};
      ok = __all(((sx[0] & 0xffffu) | (sx[1] & 0xffu)) == 0) | (--spins == 0);
    }
    const float m = u2f(sx[0]);
    float M = fmaxf(m, dpp<XOR2>(m)); M = fmaxf(M, dpp<ROR4>(M)); M = fmaxf(M, dpp<ROR8>(M));
    float wg = ex2(m - M) * u2f(sx[1]);
    PINV(wg);
    float Lg = wg + dpp<XOR2>(wg); Lg += dpp<ROR8>(Lg); Lg += dpp<ROR4>(Lg);
    *(LAS float*)(L + wwa) = wg * __builtin_amdgcn_rcpf(Lg);
  }
  wg_barrier();
  { v4u o0, o1; gather(o0, o1); SB(); project(bf, o0, o1); }
  wg_barrier();
  publish_p(pra, pxo);
  if (w >= 72) ENDWAVE();
  {
    v2u px[4], a0 = {0u, 0u}, a1 = a0, a2 = a0, a3 = a0, b0 = a0, b1 = a0, b2 = a0, b3 = a0;
    const v4u rd = {(unsigned)(size_t)ws, (unsigned)((size_t)ws >> 32) & 0xffffu, (unsigned)WS_END, 0x00020000u};
#define POLL4(r0, r1, r2, r3) asm volatile("v_mov_b64 %0, 0\n\tv_mov_b64 %1, 0\n\tv_mov_b64 %2, 0\n\tv_mov_b64 %3, 0\n\t" \
      "buffer_atomic_add_x2 %0, %4, %8, 0 offen sc0 sc1\n\tbuffer_atomic_add_x2 %1, %5, %8, 0 offen sc0 sc1\n\t" \
      "buffer_atomic_add_x2 %2, %6, %8, 0 offen sc0 sc1\n\tbuffer_atomic_add_x2 %3, %7, %8, 0 offen sc0 sc1" \
      : "+v"(r0), "+v"(r1), "+v"(r2), "+v"(r3) : "v"(rvo[0]), "v"(rvo[1]), "v"(rvo[2]), "v"(rvo[3]), "s"(rd) : "memory")
#define TAKE4(r0, r1, r2, r3) asm volatile("s_waitcnt vmcnt(4)" : "+v"(r0), "+v"(r1), "+v"(r2), "+v"(r3)::"memory"); \
      px[0] = r0 ^ tl[0]; px[1] = r1 ^ tl[1]; px[2] = r2 ^ tl[2]; px[3] = r3 ^ tl[3]; \
      if (__all(((px[0][0] | px[0][1] | px[1][0] | px[1][1] | px[2][0] | px[2][1] | px[3][0] | px[3][1]) & 0xffu) == 0)) break
    __builtin_amdgcn_s_sleep(8);
    POLL4(a0, a1, a2, a3);
    __builtin_amdgcn_s_sleep(6);
#pragma nounroll
    for (unsigned spins = 0; spins < SPIN_CAP; ++spins) {
      POLL4(b0, b1, b2, b3);
      TAKE4(a0, a1, a2, a3);
      POLL4(a0, a1, a2, a3);
      TAKE4(b0, b1, b2, b3);
    }
#undef POLL4
#undef TAKE4
    const f32x2 ya = __builtin_bit_cast(f32x2, px[0]) + __builtin_bit_cast(f32x2, px[1]), yb = __builtin_bit_cast(f32x2, px[2]) + __builtin_bit_cast(f32x2, px[3]);
    float ev = t16_sum(t32_sum(ya[0], yb[0]), t32_sum(ya[1], yb[1]));
    ev += dpp<ROR8>(ev);
    const float r = x32_sum(rbf(gt * rbf(ev)));
    *op = (unsigned short)(cvtpk(r, r) & 0xffffu);
    if (w == 0) __builtin_amdgcn_raw_buffer_store_b32(ep, rws, lane ? OOB : 0, 0, AUX_SC1);
    asm volatile("s_waitcnt vmcnt(0)" : "+v"(a0), "+v"(a1), "+v"(a2), "+v"(a3), "+v"(b0), "+v"(b1), "+v"(b2), "+v"(b3)::"memory");
  }
  ENDWAVE();
}

extern "C" void kernel_launch(void* const* d_in, const int* in_sizes, int n_in, void* d_out, int out_size, void* d_ws, size_t ws_size,
                              hipStream_t stream) {
  fused_decode<<<256, NTHR, 0, stream>>>(d_in[3], d_in[8], d_in[0], d_in[1], d_in[6], d_in[4], d_in[9], d_in[2], d_in[7], d_in[10], d_out, d_ws);
}
```

```cpp
#include <hip/hip_runtime.h>
#include <stdint.h>

#define GAS __attribute__((address_space(1)))
#define LAS __attribute__((address_space(3)))
#define CAS __attribute__((address_space(4)))
typedef unsigned v4u __attribute__((ext_vector_type(4)));
typedef unsigned v2u __attribute__((ext_vector_type(2)));
typedef float f32x4 __attribute__((ext_vector_type(4)));
typedef float f32x2 __attribute__((ext_vector_type(2)));
typedef unsigned long long v8p __attribute__((ext_vector_type(8)));
typedef unsigned long long v2p __attribute__((ext_vector_type(2)));
typedef short bf16x8 __attribute__((ext_vector_type(8)));
typedef short v4s __attribute__((ext_vector_type(4)));
typedef __bf16 bf16x2 __attribute__((ext_vector_type(2)));
typedef __amdgpu_buffer_rsrc_t rsrc_t;

constexpr int DM = 576, SK = 8192, DH = 128, WIN = 2048;
constexpr int NTHR = 640;
constexpr int AUX_NT = 2, AUX_SC1 = 16;
constexpr unsigned SPIN_CAP = 1u << 16;
constexpr float LOG2E = 1.4426950408889634f;
constexpr int W_FAST = 4, W_NORM = 3, W_SLOW = 2;
constexpr int LEAD_HI = 70, LEAD_LO = 120;
constexpr int TAU = 29;
constexpr int PUB = 21;
constexpr int NB0 = 2;
constexpr int N_FILL = 24;
constexpr int PACE_END = 76;
constexpr int DLY_Q = 500;
constexpr int CODE_BYTES = 18048;
constexpr int WS_PACE = 4096;
constexpr int WS_FL = WS_PACE + 32768;
constexpr int WS_MB = WS_FL + 32768, MBS = 3200;
constexpr int WS_QX = WS_MB;
constexpr int WS_OA = WS_MB + 512;
constexpr int WS_OB = WS_MB + 1024;
constexpr int WS_PX = WS_MB + 2048;
constexpr int WS_ML = WS_MB + 3072;
constexpr int WS_END = WS_MB + 256 * MBS;
constexpr int L_FLAG = 0;
constexpr int L_PROG = 32, L_MODE = 64;
constexpr int L_ML = 128;
constexpr int L_F = L_ML + 256;
constexpr int L_W = L_F + 128;
constexpr int L_Q = L_W + 256;
constexpr int L_QP = L_Q + 1024;
constexpr int L_PP = L_QP + 10 * 512;
constexpr int L_OP = L_PP + 2 * 4608;
constexpr int L_WO = L_OP + 16384;
constexpr int L_END = L_WO + 9 * 4608;
constexpr int LDS_BYTES = 84 * 1024;
static_assert(L_END <= LDS_BYTES, "lds");

#define VMWAIT(n) __builtin_amdgcn_s_waitcnt(((n) & 15) | (((n) >> 4) << 14) | 0x0F70)
#define LGKM0() asm volatile("s_waitcnt lgkmcnt(0)" ::: "memory")
#define SB() __builtin_amdgcn_sched_barrier(0)
#define PINV(x) asm volatile("" : "+v"(x))
#define PINS(x) asm volatile("" : "+s"(x))
#define MEMFENCE() asm volatile("" ::: "memory")
#define ENDWAVE() do { __builtin_amdgcn_endpgm(); __builtin_unreachable(); } while (0)
#define INL __attribute__((always_inline))
constexpr int OOB = 0x40000000;

__device__ __forceinline__ rsrc_t mk(const void* p, int bytes) { return __builtin_amdgcn_make_buffer_rsrc((void*)p, 0, bytes, 0x00020000); }
template <int AUX> __device__ __forceinline__ v4u ld128(rsrc_t r, int vo, int so) { return __builtin_amdgcn_raw_buffer_load_b128(r, vo, so, AUX); }
__device__ __forceinline__ void st128(v4u v, rsrc_t r, int vo, int so) {
  __builtin_amdgcn_raw_buffer_store_b128(v, r, vo, so, AUX_SC1);
  SB(); asm volatile("s_nop 1"); SB();
}
__device__ __forceinline__ f32x4 mfma(v4u a, v4u b, f32x4 c) {
  return __builtin_amdgcn_mfma_f32_16x16x32_bf16(__builtin_bit_cast(bf16x8, a), __builtin_bit_cast(bf16x8, b), c, 0, 0, 0);
}
__device__ __forceinline__ v2u ldtr(LAS unsigned char* p) { return __builtin_bit_cast(v2u, __builtin_amdgcn_ds_read_tr16_b64_v4i16((LAS v4s*)p)); }
__device__ __forceinline__ float u2f(unsigned u) { return __builtin_bit_cast(float, u); }
__device__ __forceinline__ unsigned f2u(float f) { return __builtin_bit_cast(unsigned, f); }
__device__ __forceinline__ unsigned perm(unsigned hi, unsigned lo, unsigned sel) { return __builtin_amdgcn_perm(hi, lo, sel); }
__device__ __forceinline__ unsigned cvtpk(float lo, float hi) {
  bf16x2 r; r.x = (__bf16)lo; r.y = (__bf16)hi; return __builtin_bit_cast(unsigned, r);
}
__device__ __forceinline__ float rbf(float x) { return u2f(cvtpk(x, x) & 0xffff0000u); }
__device__ __forceinline__ float ex2(float x) { return __builtin_amdgcn_exp2f(x * LOG2E); }
template <int CTRL> __device__ __forceinline__ unsigned dppu(unsigned x) { return (unsigned)__builtin_amdgcn_mov_dpp((int)x, CTRL, 0xf, 0xf, true); }
template <int CTRL> __device__ __forceinline__ float dpp(float x) { return u2f(dppu<CTRL>(f2u(x))); }
constexpr int XOR1 = 0xB1, XOR2 = 0x4E, XOR3 = 0x1B, XOR7 = 0x141, ROR8 = 0x128, ROR4 = 0x124;
__device__ __forceinline__ float x16_max(float x) { auto s = __builtin_amdgcn_permlane16_swap(f2u(x), f2u(x), false, false); return fmaxf(u2f(s[0]), u2f(s[1])); }
__device__ __forceinline__ float x32_max(float x) { auto s = __builtin_amdgcn_permlane32_swap(f2u(x), f2u(x), false, false); return fmaxf(u2f(s[0]), u2f(s[1])); }
__device__ __forceinline__ float t16_sum(float a, float b) { auto s = __builtin_amdgcn_permlane16_swap(f2u(a), f2u(b), false, false); return u2f(s[0]) + u2f(s[1]); }
__device__ __forceinline__ float t32_sum(float a, float b) { auto s = __builtin_amdgcn_permlane32_swap(f2u(a), f2u(b), false, false); return u2f(s[0]) + u2f(s[1]); }
__device__ __forceinline__ float x16_sum(float x) { return t16_sum(x, x); }
__device__ __forceinline__ float x32_sum(float x) { return t32_sum(x, x); }
__device__ __forceinline__ v4u sparse(unsigned v, const v4u& msk) { return v4u{v & msk[0], v & msk[1], v & msk[2], v & msk[3]}; }
__device__ __forceinline__ unsigned tag8(float v, unsigned T) { return perm(f2u(v), T, 0x07060500u); }

__device__ __forceinline__ void wg_barrier() { LGKM0(); __builtin_amdgcn_s_barrier(); MEMFENCE(); }
__device__ __forceinline__ void lds_wait_ge(LAS unsigned char* L, int word, unsigned want) {
#pragma nounroll
  for (unsigned spins = 0; spins < (SPIN_CAP << 4); ++spins) {
    if ((unsigned)__builtin_amdgcn_readfirstlane(*(volatile LAS unsigned*)(L + L_FLAG + word * 4)) >= want) break;
    __builtin_amdgcn_s_sleep(1);
  }
  MEMFENCE();
}
template <int WORD> __device__ __forceinline__ void lds_count(LAS unsigned char* L) {
  asm volatile("s_mov_b64 exec, 1\n\tds_add_u32 %0, %1 offset:%2\n\ts_mov_b64 exec, -1" ::"v"((unsigned)(size_t)L), "v"(1u), "n"(L_FLAG + WORD * 4) : "memory");
}
__device__ __forceinline__ unsigned now32() { return (unsigned)__builtin_amdgcn_s_memrealtime(); }

struct Stream {
  rsrc_t ka, kb, va, vb, wo;
  int kvo, vvo, ovo;
};
constexpr int aoff(int k) { return ((k & 3) << 10) | ((k >> 2) << 17); }
constexpr int boff(int k) { return ((k & 3) << 10) | (((k >> 2) & 3) << 16) | ((k >> 4) << 20); }
template <int K> __device__ __forceinline__ v4u issue(const Stream& S) {
  if constexpr (K < 8) return ld128<AUX_NT>(S.ka, S.kvo, aoff(K));
  else if constexpr (K < 40) return ld128<AUX_NT>(S.kb, S.kvo, boff(K - 8));
  else if constexpr (K < 48) return ld128<AUX_NT>(S.va, S.vvo, aoff(K - 40));
  else if constexpr (K < 80) return ld128<AUX_NT>(S.vb, S.vvo, boff(K - 48));
  else return ld128<AUX_NT>(S.wo, S.ovo, (K - 80) * 8 * 1152);
}
constexpr int id_at(int pos) { return pos < 8 ? pos : pos < 16 ? pos + 32 : pos < 48 ? pos - 8 : pos; }

struct State {
  v4u buf[84];
  v4u q[2];
  v4u msk;
  f32x4 d[4];
  float sc[10];
  unsigned pp[10];
  f32x4 acc[2];
  bool p1, p2;
  unsigned mode, mnext;
};

template <int LO, int HI> __device__ __forceinline__ void softmax(State& st, LAS unsigned char* L, int br, int wave, int lane) {
  float m = st.sc[LO];
#pragma unroll
  for (int t = LO + 1; t < HI; ++t) m = fmaxf(m, st.sc[t]);
  m = fmaxf(m, dpp<XOR1>(m)); m = fmaxf(m, dpp<XOR2>(m)); m = x16_max(m); m = x32_max(m);
  float l = 0.f;
#pragma unroll
  for (int t = LO; t < HI; ++t) {
    const float p = ex2(st.sc[t] - m);
    l += p;
    st.pp[t] = cvtpk(p, p);
  }
  l += dpp<XOR1>(l); l += dpp<XOR2>(l); l = x16_sum(l); l = x32_sum(l);
  *(LAS v2u*)(L + L_ML + ((br * 2 + ((lane >> 3) & 1)) * 8 + wave) * 8) = v2u{f2u(m), f2u(l)};
}

template <int K> __device__ __forceinline__ void process(State& st, LAS unsigned char* L, int wave, int lane) {
  if constexpr (K < 40) {
    constexpr int a = K & 3, t = K >> 2, br = t < 2 ? 0 : 1;
    st.d[a] = mfma(st.buf[K], st.q[br], f32x4{0.f, 0.f, 0.f, 0.f});
    if constexpr (a == 3) {
      float w[4];
#pragma unroll
      for (int x = 0; x < 4; ++x) {
        const float lo = st.p1 ? st.d[1 ^ x][1] : st.d[0 ^ x][0];
        const float hi = st.p1 ? st.d[3 ^ x][3] : st.d[2 ^ x][2];
        w[x] = st.p2 ? hi : lo;
      }
      const float r = (w[0] + dpp<XOR1>(w[1])) + (dpp<XOR2>(w[2]) + dpp<XOR3>(w[3]));
      st.sc[t] = rbf(rbf(r) * 0.08838834764831845f);
      if constexpr (t == 1) softmax<0, 2>(st, L, 0, wave, lane);
      if constexpr (t == 9) { softmax<2, 10>(st, L, 1, wave, lane); lds_count<2>(L); }
    }
  } else {
    constexpr int a = K & 3, t = (K - 40) >> 2, br = t < 2 ? 0 : 1;
    if constexpr (K == 79) lds_count<5>(L);
    st.acc[br] = mfma(sparse(dppu<a * 0x55>(st.pp[t]), st.msk), st.buf[K], st.acc[br]);
    if constexpr (K == 48) { *(LAS f32x4*)(L + L_OP + wave * 1024 + lane * 16) = st.acc[0]; lds_count<3>(L); }
  }
}
template <int P, int HI> __device__ __forceinline__ void proc_range(State& st, LAS unsigned char* L, int wave, int lane) {
  if constexpr (P < HI) {
    process<id_at(P)>(st, L, wave, lane);
    proc_range<P + 1, HI>(st, L, wave, lane);
  }
}
template <int I> __device__ __forceinline__ void step(State& st, const Stream& S, LAS unsigned char* L, int wave) {
  asm volatile("s_cmp_lt_u32 %0, 2\n\ts_cbranch_scc1 1f\n\ts_waitcnt vmcnt(%2)\n1:\n\ts_cmp_lt_u32 %0, 1\n\ts_cbranch_scc1 2f\n\ts_waitcnt vmcnt(%1)\n2:"
               ::"s"(st.mode), "n"(W_NORM - 1), "n"(W_SLOW - 1) : "scc", "memory");
  VMWAIT(W_FAST - 1);
  st.buf[id_at(I)] = issue<id_at(I)>(S);
  SB();
  if constexpr ((I & 1) == 0 && I <= PACE_END) {
    st.mode = __builtin_amdgcn_readfirstlane(st.mnext);
    *(volatile LAS unsigned*)(L + L_PROG + wave * 4) = I;
    st.mnext = *(volatile LAS unsigned*)(L + L_MODE + wave * 4);
  }
}
template <int I, int HI> __device__ __forceinline__ void fill(State& st, const Stream& S, LAS unsigned char* L, int wave) {
  if constexpr (I < HI) {
    step<I>(st, S, L, wave);
    SB();
    fill<I + 1, HI>(st, S, L, wave);
  }
}
constexpr int cmin(int a, int b) { return a < b ? a : b; }
template <int I, int P> __device__ __forceinline__ void mainloop(State& st, const Stream& S, LAS unsigned char* L, int wave, int lane) {
  if constexpr (I < 80) {
    step<I>(st, S, L, wave);
    constexpr int PH = cmin(P + 4, I + 1 - W_FAST);
    proc_range<P, PH>(st, L, wave, lane);
    SB();
    mainloop<I + 1, PH>(st, S, L, wave, lane);
  } else if constexpr (P < 80) {
    proc_range<P, P + 1>(st, L, wave, lane);
    SB();
    mainloop<I, P + 1>(st, S, L, wave, lane);
  }
}

__global__ void __launch_bounds__(NTHR, 3) fused_decode(const void*, const void*, const void*, const void*, const void*, const void*,
                                                        void*, void*, const void*, const void*, const void*, const void*) {
  v8p pa; v2p pb;
  asm volatile("s_load_dwordx16 %0, %2, 0x0\n\ts_load_dwordx4 %1, %2, %3" : "=&s"(pa), "=&s"(pb)
               : "s"(__builtin_amdgcn_kernarg_segment_ptr()), "s"(64 + (blockIdx.x & 4) * 4) : "memory");
  SB();
  __shared__ __attribute__((aligned(16))) unsigned char smem[LDS_BYTES];
  LAS unsigned char* L = (LAS unsigned char*)smem;
  const int tid = threadIdx.x, lane = tid & 63, wave = __builtin_amdgcn_readfirstlane(tid >> 6);
  unsigned cp0 = 0, cp1 = 0, cp2 = 0;
  if (wave == 0) {
    unsigned long long pc; asm volatile("s_getpc_b64 %0" : "=s"(pc));
    const rsrc_t rc = mk((const void*)(pc & ~127ull), CODE_BYTES);
    cp0 = __builtin_amdgcn_raw_buffer_load_b32(rc, lane * 128, 0, 0); cp1 = __builtin_amdgcn_raw_buffer_load_b32(rc, lane * 128, 8192, 0);
    cp2 = __builtin_amdgcn_raw_buffer_load_b32(rc, lane * 128, 16384, 0);
  }
  SB();
  const int bid = blockIdx.x, w = (bid & 7) * 32 + (bid >> 3);
  const int pair = w >> 3, s = w & 7, b = pair >> 4, kvh = pair & 15;
  const int g = lane >> 4, n = lane & 15, j0 = n & 7;
  const int v0 = 9 * (w & 31), v = v0 + wave, urb = (w & 96) + (v & 31);
  unsigned offa = (pair * SK + SK - WIN) * (DH * 2) + ((s & 3) << 15) + ((s >> 2) << 18), offb = pair * SK * (DH * 2) + ((s & 1) << 15) + ((s >> 1) << 18);
  unsigned offw = urb * (32 * DM * 2) + (v >> 5) * 128, offq = (s & 1) * (288 * 8192) + kvh * 512 + (s >> 1) * 128;
  int qvo = (wave * 32 + g * 8 + (n >> 3) * 4) * 8192 + j0 * 16, xvo = ((n >> 3) * DM + (s & 1) * 288 + wave * 32 + g * 8) * 2;
  unsigned tk = (pair << 21) + 0x180000 + pair * 4352, tw0 = (w & 96) * (32 * DM * 2), tw1 = tw0 + 32 * (32 * DM * 2) - 128;
  PINS(offa); PINS(offb); PINS(offw); PINS(offq); PINV(qvo); PINV(xvo); PINS(tk); PINS(tw0); PINS(tw1);
  unsigned long long t64;
  SB();
  asm volatile("s_memrealtime %2\n\ts_waitcnt lgkmcnt(0)" : "+s"(pa), "+s"(pb), "=&s"(t64)::"memory");
  SB();
  const unsigned t_start = (unsigned)t64;
  typedef const char* P;
  const P x = (P)pa[0], kA = (P)pa[1], vA = (P)pa[2], kB = (P)pa[3], vB = (P)pa[4], gw = (P)pa[5], ws = (P)pa[7], wqb = (P)pb[0], wob = (P)pb[1];
  unsigned e0, d1, d2, d3, d4;
  asm volatile("s_cmp_lg_u32 %16, 9\n\ts_cbranch_scc1 1f\n\t"
               "s_cmp_lg_u32 %17, 0\n\ts_cbranch_scc1 2f\n\t"
               "s_load_dword %1, %6, %13\n\ts_load_dword %2, %7, %13\n\ts_load_dword %3, %8, %13\n\ts_load_dword %4, %9, %13\n"
               "2:\n\ts_cmp_lg_u32 %17, 1\n\ts_cbranch_scc1 3f\n\t"
               "s_load_dword %1, %10, 0x0\n\ts_load_dword %2, %12, %14\n\ts_load_dword %3, %12, %15\n\ts_load_dword %4, %11, 0x0\n"
               "3:\n\ts_load_dword %0, %5, 0x0\n\ts_barrier\n1:"
               : "=&s"(e0), "=&s"(d1), "=&s"(d2), "=&s"(d3), "=&s"(d4)
               : "s"(ws), "s"(kA), "s"(vA), "s"(kB), "s"(vB), "s"(x), "s"(gw), "s"(wob), "s"(tk), "s"(tw0), "s"(tw1), "s"(wave), "s"(s)
               : "scc", "memory");

  if (wave == 9) {
    asm volatile("s_waitcnt lgkmcnt(0)" : "+s"(e0), "+s"(d1), "+s"(d2), "+s"(d3), "+s"(d4)::"memory");
    const rsrc_t rws = mk(ws, WS_END);
    const unsigned epp = e0 + 1u, tg = epp & 0xffffu;
    volatile LAS unsigned* prog = (volatile LAS unsigned*)(L + L_PROG) + j0;
    volatile LAS unsigned* modes = (volatile LAS unsigned*)(L + L_MODE) + j0;
    int ref = 0; bool have = false;
    unsigned until = ((t_start >> 8) + 1) << 8;
#pragma nounroll
    for (unsigned spins = 0; spins < SPIN_CAP; ++spins) {
      const int p = *prog;
      if (__any(p >= PACE_END)) break;
      const unsigned now = now32();
      if (have) {
        const int lead = (short)(p * (2 * TAU) - 2 * now - ref);
        *modes = lead > LEAD_HI ? 2u : (lead < -LEAD_LO ? 0u : 1u);
      }
      if ((int)(now - until) >= 0) {
        const int slot = WS_PACE + ((until >> 8) & 31) * 1024;
        if (until & 128) {
          MEMFENCE();
          const unsigned o = __builtin_amdgcn_raw_buffer_load_b32(rws, j0 * 128, slot, AUX_SC1);
          if (__all((o & 0xffffu) == tg)) {
            const int b0 = __builtin_amdgcn_readfirstlane(o >> 16);
            int d = (short)((o >> 16) - b0);
            d += dppu<XOR1>(d); d += dppu<XOR2>(d); d += dppu<XOR7>(d);
            ref = b0 + (d >> 3); have = true;
          }
        } else if ((bid >> 3) == PUB) {
          int sum = p + dppu<XOR1>(p); sum += dppu<XOR2>(sum); sum += dppu<XOR7>(sum);
          __builtin_amdgcn_raw_buffer_store_b32((((sum * TAU - 8 * now) >> 2) << 16) | tg, rws, lane ? OOB : (bid & 7) * 128, slot, AUX_SC1);
        }
        until += 128;
      } else __builtin_amdgcn_s_sleep(4);
    }
    __builtin_amdgcn_s_setprio(3);
    const bool need = lane < 32 && b;
    int fpo = need ? WS_FL + w * 128 + lane * 4 : OOB;
    const int u0 = (lane >> 1) * 32 + (kvh & 3) * 8, wc = (u0 + (lane & 1) * 7) / 9;
    int fvo = (lane < 18 && (!(lane & 1) || wc != u0 / 9)) ? WS_FL + (128 + (kvh >> 2) * 32 + wc) * 128 + (((9 * wc) >> 3) == (u0 >> 3) ? 0 : 64) + (b * 8 + s) * 4 : OOB;
    unsigned epv = epp, want = need ? epp : 0u;
    PINV(fpo); PINV(fvo); PINV(epv); PINV(want);
    lds_wait_ge(L, 5, 8u);
    __builtin_amdgcn_raw_buffer_store_b32(epv, rws, fvo, 0, AUX_SC1);
    wg_barrier();
    unsigned f0, f1, f2, f3, cnt = SPIN_CAP;
    const v4u rd = {(unsigned)(size_t)ws, (unsigned)((size_t)ws >> 32) & 0xffffu, (unsigned)WS_END, 0x00020000u};
#define POLL(r) "v_mov_b32 " r ", 0\n\tbuffer_atomic_add " r ", %5, %6, 0 offen sc0 sc1\n\t"
#define TEST(r) "s_waitcnt vmcnt(3)\n\tv_cmp_ne_u32 vcc, " r ", %7\n\ts_cbranch_vccz 2f\n\ts_sleep 2\n\t"
    asm volatile(POLL("%0") "s_sleep 3\n\t" POLL("%1") "s_sleep 3\n\t" POLL("%2") "s_sleep 3\n"
                 "1:\n\t" POLL("%3") TEST("%0") POLL("%0") TEST("%1") POLL("%1") TEST("%2") POLL("%2") TEST("%3")
                 "s_sub_u32 %4, %4, 1\n\ts_cmp_lg_u32 %4, 0\n\ts_cbranch_scc1 1b\n"
                 "2:\n\ts_barrier\n\ts_waitcnt vmcnt(0)"
                 : "=&v"(f0), "=&v"(f1), "=&v"(f2), "=&v"(f3), "+s"(cnt) : "v"(fpo), "s"(rd), "v"(want) : "vcc", "scc", "memory");
#undef POLL
#undef TEST
    ENDWAVE();
  }

  const rsrc_t rwq = mk(wqb + offq, 287 * 8192 + 128);
  v4u wq[4];
#pragma unroll
  for (int i = 0; i < 4; ++i) wq[i] = ld128<AUX_NT>(rwq, qvo, i * 8192);
  const v4u xr = ld128<0>(mk(x, 2 * DM * 2), xvo, 0);
  SB();
  const P kap = kA + offa, wop = wob + offw;
  constexpr int A_SPAN = aoff(7) + 7 * 4096 + 1024, B_SPAN = boff(31) + 7 * 4096 + 1024, WO_SPAN = 31 * 1152 + 128;
  Stream S;
  S.kvo = wave * 4096 + (n >> 2) * 256 + g * 64 + (n & 3) * 16;
  S.ovo = (lane >> 3) * 1152 + j0 * 16;
  State st;
  if (tid >= 512 && tid < 536) *(LAS unsigned*)(L + L_FLAG + lane * 4) = lane < 16 ? 0u : 1u;
  {
    const rsrc_t r0 = mk(wave < 8 ? kap : wop, wave < 8 ? A_SPAN : WO_SPAN);
    const int vo0 = wave < 8 ? S.kvo : S.ovo, st0 = wave < 8 ? 1024 : 8 * 1152;
    SB(); wg_barrier(); SB();
#pragma unroll
    for (int i = 0; i < NB0; ++i) st.buf[i] = ld128<AUX_NT>(r0, vo0, i * st0);
  }
  SB();
  const rsrc_t rws = mk(ws, WS_END);
  S.ka = mk(kap, A_SPAN); S.va = mk(vA + offa, A_SPAN);
  S.kb = mk(kB + offb, B_SPAN); S.vb = mk(vB + offb, B_SPAN);
  S.wo = mk(wop, WO_SPAN);
  S.vvo = wave * 4096 + lane * 16;
#pragma unroll
  for (int i = 0; i < 4; ++i) st.msk[i] = ((j0 >> 1) == i) ? ((j0 & 1) ? 0xffff0000u : 0x0000ffffu) : 0u;
  st.p1 = lane & 1; st.p2 = lane & 2;
  {
    f32x4 d0 = {0.f, 0.f, 0.f, 0.f}, d1 = {0.f, 0.f, 0.f, 0.f};
#pragma unroll
    for (int i = 0; i < 4; ++i) {
      const unsigned sel = (i & 1) ? 0x03020302u : 0x01000100u;
      d0 = mfma(sparse(perm(xr[i >> 1], xr[i >> 1], sel), st.msk), wq[i], d0);
      d1 = mfma(sparse(perm(xr[2 + (i >> 1)], xr[2 + (i >> 1)], sel), st.msk), wq[i], d1);
    }
    f32x4 y;
#pragma unroll
    for (int i = 0; i < 4; ++i) y[i] = d0[i] + dpp<ROR8>(d1[i]);
    *(LAS f32x4*)(L + L_QP + (n < 8 ? wave : 9) * 512 + (g >> 1) * 256 + j0 * 32 + (g & 1) * 16) = y;
    lds_count<0>(L);
    asm volatile("" ::"v"(cp0), "v"(cp1), "v"(cp2));
    SB();
  }
  unsigned ep, T, T16;
  auto wo_frags = [&](const v4u w0, const v4u w1, const v4u w2, const v4u w3, v4u (&bf)[4]) INL {
    LAS unsigned char* wr = L + L_WO + wave * 4608 + (lane >> 3) * 144 + j0 * 16;
    *(LAS v4u*)(wr) = w0; *(LAS v4u*)(wr + 8 * 144) = w1; *(LAS v4u*)(wr + 16 * 144) = w2; *(LAS v4u*)(wr + 24 * 144) = w3;
    MEMFENCE();
    LAS unsigned char* rd = L + L_WO + wave * 4608 + (g * 8 + (n >> 2)) * 144 + (n & 3) * 8;
#pragma unroll
    for (int t = 0; t < 4; ++t) {
      const v2u lo = ldtr(rd + t * 32), hi = ldtr(rd + 4 * 144 + t * 32);
      bf[t] = v4u{lo[0], lo[1], hi[0], hi[1]};
    }
    PINV(bf[0]); PINV(bf[1]); PINV(bf[2]); PINV(bf[3]);
    SB();
  };
  int ovo = ((urb >> 3) * 8 + (lane >> 3)) * MBS + (b ? WS_OB + (urb & 7) * 128 + j0 * 16 : WS_OA + (urb & 7) * 64 + (j0 >> 1) * 16);
  unsigned psel = b ? 0x07060302u : (j0 & 1) ? 0x07060504u : 0x03020100u, tmask = b ? 0xffffu : 0u;
  int awa = L_WO + wave * 4608 + (lane >> 3) * 80 + j0 * 8, ara = L_WO + wave * 4608 + n * 80 + g * 16;
  int wra = L_W + (((v >> 3) - (v0 >> 3)) * 2 + ((urb >> 2) & 1)) * 64 + g * 16;
  const int cut = 32 - (v0 & 31);
  int pwa = L_PP + (wave >= cut ? 4608 : 0) + wave * 512 + (lane >> 5) * 256 + (lane & 31) * 4;
  auto gather = [&](v4u& o0, v4u& o1) INL { o0 = ld128<AUX_SC1>(rws, ovo, 0); o1 = ld128<AUX_SC1>(rws, ovo, 128 * MBS); };
  auto project = [&](const v4u (&bf)[4], v4u o0, v4u o1) INL {
    const f32x4 wt = *(LAS f32x4*)(L + wra);
    SB();
    v4u a;
    auto take = [&]() INL -> bool {
      *(LAS v2u*)(L + awa) = v2u{perm(o0[2], o0[0], psel), perm(o0[3], o0[1], psel)};
      *(LAS v2u*)(L + awa + 640) = v2u{perm(o1[2], o1[0], psel), perm(o1[3], o1[1], psel)};
      MEMFENCE();
      a = *(LAS v4u*)(L + ara);
      SB();
      const unsigned b0 = __builtin_amdgcn_bitop3_b32(o0[0], o0[1], T16, 0x7e), b1 = __builtin_amdgcn_bitop3_b32(o0[2], o0[3], T16, 0x7e);
      const unsigned b2 = __builtin_amdgcn_bitop3_b32(o1[0], o1[1], T16, 0x7e), b3 = __builtin_amdgcn_bitop3_b32(o1[2], o1[3], T16, 0x7e);
      return __all(((b0 | b1 | b2 | b3) & tmask) == 0);
    };
    unsigned spins = SPIN_CAP;
    bool ok = take();
#pragma nounroll
    while (!ok) {
      __builtin_amdgcn_s_sleep(2);
      MEMFENCE();
      gather(o0, o1);
      ok = take() | (--spins == 0);
    }
    f32x4 d[4];
#pragma unroll
    for (int t = 0; t < 4; ++t) d[t] = mfma(a, bf[t], f32x4{0.f, 0.f, 0.f, 0.f});
    SB();
    float sm[4];
#pragma unroll
    for (int t = 0; t < 4; ++t) { const f32x2 h = f32x2{wt[0], wt[1]} * f32x2{d[t][0], d[t][1]} + f32x2{wt[2], wt[3]} * f32x2{d[t][2], d[t][3]}; sm[t] = h[0] + h[1]; }
    *(LAS float*)(L + pwa) = t16_sum(sm[0], sm[1]);
    *(LAS float*)(L + pwa + 128) = t16_sum(sm[2], sm[3]);
  };
  auto publish_p = [&](const int pra, const int pxo) INL {
    f32x2 p[9];
#pragma unroll
    for (int j = 0; j < 9; ++j) p[j] = *(LAS f32x2*)(L + pra + j * 512);
    const f32x2 y = ((p[0] + p[1]) + (p[2] + p[3])) + ((p[4] + p[5]) + (p[6] + p[7] + p[8]));
    __builtin_amdgcn_raw_buffer_store_b64(v2u{tag8(y[0], T), tag8(y[1], T)}, rws, pxo, 0, AUX_SC1);
  };

  if (__builtin_expect(wave < 8, 1)) {
    st.mode = 1; st.mnext = 1;
    fill<NB0, N_FILL>(st, S, L, wave);
    lds_wait_ge(L, 1, 1u);
    ep = __builtin_amdgcn_readfirstlane(*(LAS unsigned*)(L + L_FLAG + 16)); T = ep & 0xffu; T16 = ep & 0xffffu;
    st.q[0] = *(LAS v4u*)(L + L_Q + (n >> 3) * 256 + (g * 4 + (n & 3)) * 16);
    st.q[1] = *(LAS v4u*)(L + L_Q + 512 + (n >> 3) * 256 + (g * 4 + (n & 3)) * 16);
    st.acc[0] = f32x4{0.f, 0.f, 0.f, 0.f}; st.acc[1] = f32x4{0.f, 0.f, 0.f, 0.f};
    int pra = L_PP + 4608 + lane * 8, pxo = WS_PX + w * MBS + 512 + lane * 8;
    PINV(ovo); PINV(pwa); PINV(T16); PINV(awa); PINV(ara); PINV(wra); PINV(psel); PINV(tmask); PINV(pra); PINV(pxo);
    mainloop<N_FILL, 0>(st, S, L, wave, lane);
    *(LAS f32x4*)(L + L_OP + 8192 + wave * 1024 + lane * 16) = st.acc[1];
    SB();
    st.buf[80] = issue<80>(S); st.buf[81] = issue<81>(S); st.buf[82] = issue<82>(S); st.buf[83] = issue<83>(S);
    SB();
    wg_barrier();
    wg_barrier();
    v4u bf[4], o0, o1;
    gather(o0, o1);
    SB();
    wo_frags(st.buf[80], st.buf[81], st.buf[82], st.buf[83], bf);
    project(bf, o0, o1);
    wg_barrier();
    if (wave == 0 && cut < 9) publish_p(pra, pxo);
    return;
  }

  __builtin_amdgcn_s_setprio(3);
  static_assert(NB0 == 2, "the rest of the control wave's wo tile");
  st.buf[2] = issue<82>(S); st.buf[3] = issue<83>(S);
  ep = *(const CAS unsigned*)ws + 1u;
  PINS(ep);
  T = ep & 0xffu; T16 = ep & 0xffffu;
  *(LAS unsigned*)(L + L_FLAG + 16) = ep;
  v4u bf[4];
  lds_wait_ge(L, 0, 9u);
  {
    f32x4 qs = *(LAS f32x4*)(L + L_QP + (lane & 31) * 16);
#pragma unroll
    for (int i = 1; i < 9; ++i) qs += *(LAS f32x4*)(L + L_QP + i * 512 + (lane & 31) * 16);
    qs = __builtin_bit_cast(f32x4, __builtin_bit_cast(v4u, qs) + 0x80u);
    st128(v4u{tag8(qs[0], T), tag8(qs[1], T), tag8(qs[2], T), tag8(qs[3], T)}, rws, lane < 32 ? lane * 16 : OOB, WS_QX + w * MBS);
  }
  wo_frags(st.buf[0], st.buf[1], st.buf[2], st.buf[3], bf);
#pragma nounroll
  for (unsigned spins = 0; spins < 4096 && (int)(now32() - t_start) < DLY_Q; ++spins) __builtin_amdgcn_s_sleep(2);
  {
    v4u qx[4];
    const int gvo = g * 2 * MBS + n * 16, gso = WS_QX + kvh * 8 * MBS + b * 256;
#pragma nounroll
    for (unsigned spins = 0; spins < SPIN_CAP; ++spins) {
      MEMFENCE();
#pragma unroll
      for (int i = 0; i < 4; ++i) qx[i] = ld128<AUX_SC1>(rws, gvo, gso + ((i >> 1) * 128 + (i & 1)) * MBS) ^ T;
      const v4u bad = qx[0] | qx[1] | qx[2] | qx[3];
      if (__all(((bad[0] | bad[1] | bad[2] | bad[3]) & 0xffu) == 0)) break;
      __builtin_amdgcn_s_sleep(16);
    }
#pragma unroll
    for (int br = 0; br < 2; ++br) {
      const f32x4 v = __builtin_bit_cast(f32x4, qx[br * 2]) + __builtin_bit_cast(f32x4, qx[br * 2 + 1]);
      *(LAS v2u*)(L + L_Q + br * 512 + lane * 8) = v2u{cvtpk(v[0], v[1]), cvtpk(v[2], v[3])};
    }
    lds_count<1>(L);
  }
  float gt = 0.f;
  int rvo[5] = {OOB, OOB, OOB, OOB, OOB};
  if (w < 72) {
    const rsrc_t rg = mk(gw, DM * 2), rxr = mk(x + ((w >> 2) & 1) * (DM * 2), DM * 2);
    float z = 0.f;
#pragma unroll
    for (int i = 0; i < 5; ++i) {
      const unsigned gg = __builtin_amdgcn_raw_buffer_load_b32(rg, (i * 64 + lane) * 4, 0, 0), xx = __builtin_amdgcn_raw_buffer_load_b32(rxr, (i * 64 + lane) * 4, 0, 0);
      z += rbf(u2f(xx << 16) * u2f(gg << 16)) + rbf(u2f(xx & 0xffff0000u) * u2f(gg & 0xffff0000u));
    }
    z = x32_sum(z); z = x16_sum(z); z += dpp<ROR8>(z); z += dpp<ROR4>(z); z += dpp<XOR2>(z); z += dpp<XOR1>(z);
    gt = rbf(1.f / (1.f + __expf(-rbf(z))));
    gt = (lane & 32) ? rbf(1.f - gt) : gt;
#pragma unroll
    for (int i = 0; i < 5; ++i) {
      const int c = w >> 3, j = (32 * c) / 9 + i;
      rvo[i] = 9 * j <= 32 * c + 31 ? WS_PX + ((lane >> 3) * 32 + j) * MBS + (((9 * j) >> 5) == c ? 0 : 512) + (w & 7) * 64 + j0 * 8 : OOB;
    }
  }
  unsigned tv = T, t4 = rvo[4] == OOB ? 0u : T;
#pragma unroll
  for (int i = 0; i < 5; ++i) PINV(rvo[i]);
  PINV(tv); PINV(t4);
  PINV(gt);
  lds_wait_ge(L, 2, 8u);
  v2u mlw;
  {
    const v2u ml = *(LAS v2u*)(L + L_ML + (lane & 31) * 8);
    const float m = u2f(ml[0]);
    float M = fmaxf(m, dpp<XOR1>(m)); M = fmaxf(M, dpp<XOR2>(M)); M = fmaxf(M, dpp<XOR7>(M));
    const float e = ex2(m - M);
    float Ls = e * u2f(ml[1]);
    PINV(Ls);
    Ls += dpp<XOR1>(Ls); Ls += dpp<XOR2>(Ls); Ls += dpp<XOR7>(Ls);
    Ls = u2f(f2u(Ls) & 0xffffff00u);
    *(LAS float*)(L + L_F + (lane & 31) * 4) = e / Ls;
    mlw = v2u{(f2u(M) & 0xffff0000u) | T16, f2u(Ls) | T};
    MEMFENCE();
  }
  auto combine = [&](const int br, const f32x4 f0, const f32x4 f1) INL -> f32x4 {
    f32x4 op[8];
#pragma unroll
    for (int i = 0; i < 8; ++i) op[i] = *(LAS f32x4*)(L + L_OP + br * 8192 + i * 1024 + lane * 16);
    f32x4 O = f0[0] * op[0];
    O += f0[1] * op[1]; O += f0[2] * op[2]; O += f0[3] * op[3];
    O += f1[0] * op[4]; O += f1[1] * op[5]; O += f1[2] * op[6]; O += f1[3] * op[7];
    return O;
  };
  const int dpos = (g >> 1) * 128 + n * 8 + (g & 1) * 4, fa = L_F + (g >> 1) * 32;
  lds_wait_ge(L, 3, 8u);
  {
    const f32x4 O = combine(0, *(LAS f32x4*)(L + fa), *(LAS f32x4*)(L + fa + 16));
    __builtin_amdgcn_raw_buffer_store_b64(v2u{cvtpk(O[0], O[1]), cvtpk(O[2], O[3])}, rws, dpos * 2, WS_OA + w * MBS, AUX_SC1);
    asm volatile("s_waitcnt vmcnt(0)" ::: "memory");
    __builtin_amdgcn_raw_buffer_store_b64(mlw, rws, (lane & 0x27) ? OOB : lane, WS_ML + w * MBS, AUX_SC1);
  }
  f32x4 fb0 = *(LAS f32x4*)(L + fa + 64), fb1 = *(LAS f32x4*)(L + fa + 80);
  if (cut < 9) {
#pragma unroll
    for (int j = 0; j < 9; ++j) *(LAS v4u*)(L + L_PP + j * 1024 + lane * 16) = v4u{0u, 0u, 0u, 0u};
  }
  int svo = WS_ML + ((g & 1) * 128 + (w & 96) + ((v0 + (lane >> 5) * 8) & 24) + ((lane >> 1) & 7)) * MBS + b * 16 + (lane & 1) * 8;
  int wwa = L_W + (((lane >> 5) * 2 + (lane & 1)) * 16 + ((lane >> 1) & 15)) * 4;
  int obo = WS_OB + w * MBS + dpos * 4, pxo = WS_PX + w * MBS + lane * 8, pra = L_PP + lane * 8;
  GAS unsigned short* op = (GAS unsigned short*)pa[6] + ((w >> 2) & 1) * DM + (w >> 3) * 64 + (w & 3) * 16 + j0 * 2 + (g & 1);
  unsigned sl = 0x05040100u, sh = 0x07060100u;
  PINV(awa); PINV(ara); PINV(wra); PINV(psel); PINV(tmask); PINV(ovo); PINV(pwa); PINV(T16);
  PINV(fb0); PINV(fb1); PINV(obo); PINV(pxo); PINV(pra); PINV(op); PINV(svo); PINV(wwa); PINV(sl); PINV(sh);
  VMWAIT(0);
  wg_barrier();
  v2u sx = __builtin_amdgcn_raw_buffer_load_b64(rws, svo, 0, AUX_SC1);
  SB();
  {
    const f32x4 O = combine(1, fb0, fb1);
    const unsigned c0 = cvtpk(O[0], O[1]), c1 = cvtpk(O[2], O[3]);
    st128(v4u{perm(c0, T16, sl), perm(c1, T16, sl), perm(c0, T16, sh), perm(c1, T16, sh)}, rws, obo, 0);
  }
  {
    sx ^= v2u{T16, T};
    unsigned spins = SPIN_CAP;
    bool ok = __all(((sx[0] & 0xffffu) | (sx[1] & 0xffu)) == 0);
#pragma nounroll
    while (!ok) {
      __builtin_amdgcn_s_sleep(8);
      MEMFENCE();
      sx = __builtin_amdgcn_raw_buffer_load_b64(rws, svo, 0, AUX_SC1) ^ v2u{T16, T};
      ok = __all(((sx[0] & 0xffffu) | (sx[1] & 0xffu)) == 0) | (--spins == 0);
    }
    const float m = u2f(sx[0]);
    float M = fmaxf(m, dpp<XOR2>(m)); M = fmaxf(M, dpp<ROR4>(M)); M = fmaxf(M, dpp<ROR8>(M));
    float wg = ex2(m - M) * u2f(sx[1]);
    PINV(wg);
    float Lg = wg + dpp<XOR2>(wg); Lg += dpp<ROR8>(Lg); Lg += dpp<ROR4>(Lg);
    *(LAS float*)(L + wwa) = wg * __builtin_amdgcn_rcpf(Lg);
  }
  wg_barrier();
  { v4u o0, o1; gather(o0, o1); SB(); project(bf, o0, o1); }
  wg_barrier();
  publish_p(pra, pxo);
  if (w >= 72) ENDWAVE();
  {
    v2u px[5], a0 = {0u, 0u}, a1 = a0, a2 = a0, a3 = a0, a4 = a0, b0 = a0, b1 = a0, b2 = a0, b3 = a0, b4 = a0;
    const v4u rd = {(unsigned)(size_t)ws, (unsigned)((size_t)ws >> 32) & 0xffffu, (unsigned)WS_END, 0x00020000u};
#define POLL5(r0, r1, r2, r3, r4) asm volatile("v_mov_b64 %0, 0\n\tv_mov_b64 %1, 0\n\tv_mov_b64 %2, 0\n\tv_mov_b64 %3, 0\n\tv_mov_b64 %4, 0\n\t" \
      "buffer_atomic_add_x2 %0, %5, %10, 0 offen sc0 sc1\n\tbuffer_atomic_add_x2 %1, %6, %10, 0 offen sc0 sc1\n\t" \
      "buffer_atomic_add_x2 %2, %7, %10, 0 offen sc0 sc1\n\tbuffer_atomic_add_x2 %3, %8, %10, 0 offen sc0 sc1\n\t" \
      "buffer_atomic_add_x2 %4, %9, %10, 0 offen sc0 sc1" \
      : "+v"(r0), "+v"(r1), "+v"(r2), "+v"(r3), "+v"(r4) : "v"(rvo[0]), "v"(rvo[1]), "v"(rvo[2]), "v"(rvo[3]), "v"(rvo[4]), "s"(rd) : "memory")
#define TAKE5(r0, r1, r2, r3, r4) asm volatile("s_waitcnt vmcnt(5)" : "+v"(r0), "+v"(r1), "+v"(r2), "+v"(r3), "+v"(r4)::"memory"); \
      px[0] = r0 ^ tv; px[1] = r1 ^ tv; px[2] = r2 ^ tv; px[3] = r3 ^ tv; px[4] = r4 ^ t4; \
      if (__all(((px[0][0] | px[0][1] | px[1][0] | px[1][1] | px[2][0] | px[2][1] | px[3][0] | px[3][1] | px[4][0] | px[4][1]) & 0xffu) == 0)) break
    __builtin_amdgcn_s_sleep(8);
    POLL5(a0, a1, a2, a3, a4);
    __builtin_amdgcn_s_sleep(6);
#pragma nounroll
    for (unsigned spins = 0; spins < SPIN_CAP; ++spins) {
      POLL5(b0, b1, b2, b3, b4);
      TAKE5(a0, a1, a2, a3, a4);
      POLL5(a0, a1, a2, a3, a4);
      TAKE5(b0, b1, b2, b3, b4);
    }
#undef POLL5
#undef TAKE5
    const f32x2 y = (__builtin_bit_cast(f32x2, px[0]) + __builtin_bit_cast(f32x2, px[1])) + (__builtin_bit_cast(f32x2, px[2]) + __builtin_bit_cast(f32x2, px[3])) + __builtin_bit_cast(f32x2, px[4]);
    float ev = t16_sum(y[0], y[1]);
    ev += dpp<ROR8>(ev);
    const float r = x32_sum(rbf(gt * rbf(ev)));
    *op = (unsigned short)(cvtpk(r, r) & 0xffffu);
    if (w == 0) __builtin_amdgcn_raw_buffer_store_b32(ep, rws, lane ? OOB : 0, 0, AUX_SC1);
    asm volatile("s_waitcnt vmcnt(0)" : "+v"(a0), "+v"(a1), "+v"(a2), "+v"(a3), "+v"(a4), "+v"(b0), "+v"(b1), "+v"(b2), "+v"(b3), "+v"(b4)::"memory");
  }
  ENDWAVE();
}

extern "C" void kernel_launch(void* const* d_in, const int* in_sizes, int n_in, void* d_out, int out_size, void* d_ws, size_t ws_size,
                              hipStream_t stream) {
  fused_decode<<<256, NTHR, 0, stream>>>(d_in[0], d_in[1], d_in[2], d_in[6], d_in[7], d_in[10], d_out, d_ws, d_in[3], d_in[4], d_in[8], d_in[9]);
}
```

```cpp
#include <hip/hip_runtime.h>
#include <stdint.h>

#define GAS __attribute__((address_space(1)))
#define LAS __attribute__((address_space(3)))
#define CAS __attribute__((address_space(4)))
typedef unsigned v4u __attribute__((ext_vector_type(4)));
typedef unsigned v2u __attribute__((ext_vector_type(2)));
typedef float f32x4 __attribute__((ext_vector_type(4)));
typedef float f32x2 __attribute__((ext_vector_type(2)));
typedef unsigned long long v8p __attribute__((ext_vector_type(8)));
typedef unsigned long long v2p __attribute__((ext_vector_type(2)));
typedef short bf16x8 __attribute__((ext_vector_type(8)));
typedef short v4s __attribute__((ext_vector_type(4)));
typedef __bf16 bf16x2 __attribute__((ext_vector_type(2)));
typedef __amdgpu_buffer_rsrc_t rsrc_t;

constexpr int DM = 576, SK = 8192, DH = 128, WIN = 2048;
constexpr int NTHR = 640;
constexpr int AUX_NT = 2, AUX_SC1 = 16;
constexpr unsigned SPIN_CAP = 1u << 16;
constexpr float LOG2E = 1.4426950408889634f;
constexpr int W_FAST = 4, W_NORM = 3, W_SLOW = 2;
constexpr int LEAD_HI = 70, LEAD_LO = 120;
constexpr int TAU = 29;
constexpr int PUB = 21;
constexpr int NB0 = 2;
constexpr int N_FILL = 24;
constexpr int PACE_END = 76;
constexpr int STAT_AT = 66;
constexpr int DUE_OB = 142, DUE_OWN = 40;
constexpr int DUE_PX = 85;
constexpr int DLY_Q = 500;
constexpr int CODE_BYTES = 17920;
constexpr int WS_PACE = 4096;
constexpr int WS_MB = WS_PACE + 32768, MBS = 3200;
constexpr int WS_QX = WS_MB;
constexpr int WS_OA = WS_MB + 512;
constexpr int WS_OB = WS_MB + 1024;
constexpr int WS_PX = WS_MB + 2048;
constexpr int WS_ML = WS_MB + 3072;
constexpr int WS_END = WS_MB + 256 * MBS;
constexpr int L_FLAG = 0;
constexpr int L_PROG = 32, L_MODE = 64;
constexpr int L_ML = 128;
constexpr int L_F = L_ML + 256;
constexpr int L_W = L_F + 128;
constexpr int L_Q = L_W + 256;
constexpr int L_QP = L_Q + 1024;
constexpr int L_PP = L_QP + 10 * 512;
constexpr int L_OP = L_PP + 2 * 4608;
constexpr int L_WO = L_OP + 16384;
constexpr int L_A = L_WO + 9 * 4608;
constexpr int L_END = L_A + 9 * 1280;
constexpr int LDS_BYTES = 88 * 1024;
static_assert(L_END <= LDS_BYTES, "lds");

#define VMWAIT(n) __builtin_amdgcn_s_waitcnt(((n) & 15) | (((n) >> 4) << 14) | 0x0F70)
#define LGKM0() asm volatile("s_waitcnt lgkmcnt(0)" ::: "memory")
#define SB() __builtin_amdgcn_sched_barrier(0)
#define PINV(x) asm volatile("" : "+v"(x))
#define PINS(x) asm volatile("" : "+s"(x))
#define MEMFENCE() asm volatile("" ::: "memory")
#define ENDWAVE() do { __builtin_amdgcn_endpgm(); __builtin_unreachable(); } while (0)
#define INL __attribute__((always_inline))
constexpr int OOB = 0x40000000;

__device__ __forceinline__ rsrc_t mk(const void* p, int bytes) { return __builtin_amdgcn_make_buffer_rsrc((void*)p, 0, bytes, 0x00020000); }
template <int AUX> __device__ __forceinline__ v4u ld128(rsrc_t r, int vo, int so) { return __builtin_amdgcn_raw_buffer_load_b128(r, vo, so, AUX); }
__device__ __forceinline__ void st128(v4u v, rsrc_t r, int vo, int so) {
  __builtin_amdgcn_raw_buffer_store_b128(v, r, vo, so, AUX_SC1);
  SB(); asm volatile("s_nop 1"); SB();
}
__device__ __forceinline__ f32x4 mfma(v4u a, v4u b, f32x4 c) {
  return __builtin_amdgcn_mfma_f32_16x16x32_bf16(__builtin_bit_cast(bf16x8, a), __builtin_bit_cast(bf16x8, b), c, 0, 0, 0);
}
__device__ __forceinline__ v2u ldtr(LAS unsigned char* p) { return __builtin_bit_cast(v2u, __builtin_amdgcn_ds_read_tr16_b64_v4i16((LAS v4s*)p)); }
__device__ __forceinline__ float u2f(unsigned u) { return __builtin_bit_cast(float, u); }
__device__ __forceinline__ unsigned f2u(float f) { return __builtin_bit_cast(unsigned, f); }
__device__ __forceinline__ unsigned perm(unsigned hi, unsigned lo, unsigned sel) { return __builtin_amdgcn_perm(hi, lo, sel); }
__device__ __forceinline__ unsigned cvtpk(float lo, float hi) {
  bf16x2 r; r.x = (__bf16)lo; r.y = (__bf16)hi; return __builtin_bit_cast(unsigned, r);
}
__device__ __forceinline__ float rbf(float x) { return u2f(cvtpk(x, x) & 0xffff0000u); }
__device__ __forceinline__ float ex2(float x) { return __builtin_amdgcn_exp2f(x * LOG2E); }
template <int CTRL> __device__ __forceinline__ unsigned dppu(unsigned x) { return (unsigned)__builtin_amdgcn_mov_dpp((int)x, CTRL, 0xf, 0xf, true); }
template <int CTRL> __device__ __forceinline__ float dpp(float x) { return u2f(dppu<CTRL>(f2u(x))); }
constexpr int XOR1 = 0xB1, XOR2 = 0x4E, XOR3 = 0x1B, XOR7 = 0x141, ROR8 = 0x128, ROR4 = 0x124;
__device__ __forceinline__ float x16_max(float x) { auto s = __builtin_amdgcn_permlane16_swap(f2u(x), f2u(x), false, false); return fmaxf(u2f(s[0]), u2f(s[1])); }
__device__ __forceinline__ float x32_max(float x) { auto s = __builtin_amdgcn_permlane32_swap(f2u(x), f2u(x), false, false); return fmaxf(u2f(s[0]), u2f(s[1])); }
__device__ __forceinline__ float t16_sum(float a, float b) { auto s = __builtin_amdgcn_permlane16_swap(f2u(a), f2u(b), false, false); return u2f(s[0]) + u2f(s[1]); }
__device__ __forceinline__ float t32_sum(float a, float b) { auto s = __builtin_amdgcn_permlane32_swap(f2u(a), f2u(b), false, false); return u2f(s[0]) + u2f(s[1]); }
__device__ __forceinline__ float x16_sum(float x) { return t16_sum(x, x); }
__device__ __forceinline__ float x32_sum(float x) { return t32_sum(x, x); }
__device__ __forceinline__ v4u sparse(unsigned v, const v4u& msk) { return v4u{v & msk[0], v & msk[1], v & msk[2], v & msk[3]}; }
__device__ __forceinline__ unsigned tag8(float v, unsigned T) { return perm(f2u(v), T, 0x07060500u); }

__device__ __forceinline__ void wg_barrier() { LGKM0(); __builtin_amdgcn_s_barrier(); MEMFENCE(); }
__device__ __forceinline__ void lds_wait_ge(LAS unsigned char* L, int word, unsigned want) {
#pragma nounroll
  for (unsigned spins = 0; spins < (SPIN_CAP << 4); ++spins) {
    if ((unsigned)__builtin_amdgcn_readfirstlane(*(volatile LAS unsigned*)(L + L_FLAG + word * 4)) >= want) break;
    __builtin_amdgcn_s_sleep(1);
  }
  MEMFENCE();
}
template <int WORD> __device__ __forceinline__ void lds_count(LAS unsigned char* L) {
  asm volatile("s_mov_b64 exec, 1\n\tds_add_u32 %0, %1 offset:%2\n\ts_mov_b64 exec, -1" ::"v"((unsigned)(size_t)L), "v"(1u), "n"(L_FLAG + WORD * 4) : "memory");
}
__device__ __forceinline__ unsigned now32() { return (unsigned)__builtin_amdgcn_s_memrealtime(); }

struct Stream {
  rsrc_t ka, kb, va, vb, wo;
  int kvo, vvo, ovo;
};
constexpr int aoff(int k) { return ((k & 3) << 10) | ((k >> 2) << 17); }
constexpr int boff(int k) { return ((k & 3) << 10) | (((k >> 2) & 3) << 16) | ((k >> 4) << 20); }
template <int K> __device__ __forceinline__ v4u issue(const Stream& S) {
  if constexpr (K < 8) return ld128<AUX_NT>(S.ka, S.kvo, aoff(K));
  else if constexpr (K < 40) return ld128<AUX_NT>(S.kb, S.kvo, boff(K - 8));
  else if constexpr (K < 48) return ld128<AUX_NT>(S.va, S.vvo, aoff(K - 40));
  else if constexpr (K < 80) return ld128<AUX_NT>(S.vb, S.vvo, boff(K - 48));
  else return ld128<AUX_NT>(S.wo, S.ovo, (K - 80) * 8 * 1152);
}
constexpr int id_at(int pos) { return pos < 8 ? pos : pos < 16 ? pos + 32 : pos < 48 ? pos - 8 : pos; }

struct State {
  v4u buf[84];
  v4u q[2];
  v4u msk;
  f32x4 d[4];
  float sc[10];
  unsigned pp[10];
  f32x4 acc[2];
  bool p1, p2;
  unsigned mode, mnext;
};

template <int LO, int HI> __device__ __forceinline__ void softmax(State& st, LAS unsigned char* L, int br, int wave, int lane) {
  float m = st.sc[LO];
#pragma unroll
  for (int t = LO + 1; t < HI; ++t) m = fmaxf(m, st.sc[t]);
  m = fmaxf(m, dpp<XOR1>(m)); m = fmaxf(m, dpp<XOR2>(m)); m = x16_max(m); m = x32_max(m);
  float l = 0.f;
#pragma unroll
  for (int t = LO; t < HI; ++t) {
    const float p = ex2(st.sc[t] - m);
    l += p;
    st.pp[t] = cvtpk(p, p);
  }
  l += dpp<XOR1>(l); l += dpp<XOR2>(l); l = x16_sum(l); l = x32_sum(l);
  *(LAS v2u*)(L + L_ML + ((br * 2 + ((lane >> 3) & 1)) * 8 + wave) * 8) = v2u{f2u(m), f2u(l)};
}

template <int K> __device__ __forceinline__ void process(State& st, LAS unsigned char* L, int wave, int lane) {
  if constexpr (K < 40) {
    constexpr int a = K & 3, t = K >> 2, br = t < 2 ? 0 : 1;
    st.d[a] = mfma(st.buf[K], st.q[br], f32x4{0.f, 0.f, 0.f, 0.f});
    if constexpr (a == 3) {
      float w[4];
#pragma unroll
      for (int x = 0; x < 4; ++x) {
        const float lo = st.p1 ? st.d[1 ^ x][1] : st.d[0 ^ x][0];
        const float hi = st.p1 ? st.d[3 ^ x][3] : st.d[2 ^ x][2];
        w[x] = st.p2 ? hi : lo;
      }
      const float r = (w[0] + dpp<XOR1>(w[1])) + (dpp<XOR2>(w[2]) + dpp<XOR3>(w[3]));
      st.sc[t] = rbf(rbf(r) * 0.08838834764831845f);
      if constexpr (t == 1) softmax<0, 2>(st, L, 0, wave, lane);
      if constexpr (t == 9) { softmax<2, 10>(st, L, 1, wave, lane); lds_count<2>(L); }
    }
  } else {
    constexpr int a = K & 3, t = (K - 40) >> 2, br = t < 2 ? 0 : 1;
    st.acc[br] = mfma(sparse(dppu<a * 0x55>(st.pp[t]), st.msk), st.buf[K], st.acc[br]);
    if constexpr (K == 48) { *(LAS f32x4*)(L + L_OP + wave * 1024 + lane * 16) = st.acc[0]; lds_count<3>(L); }
  }
}
template <int P, int HI> __device__ __forceinline__ void proc_range(State& st, LAS unsigned char* L, int wave, int lane) {
  if constexpr (P < HI) {
    process<id_at(P)>(st, L, wave, lane);
    proc_range<P + 1, HI>(st, L, wave, lane);
  }
}
template <int I> __device__ __forceinline__ void step(State& st, const Stream& S, LAS unsigned char* L, int wave) {
  asm volatile("s_cmp_lt_u32 %0, 2\n\ts_cbranch_scc1 1f\n\ts_waitcnt vmcnt(%2)\n1:\n\ts_cmp_lt_u32 %0, 1\n\ts_cbranch_scc1 2f\n\ts_waitcnt vmcnt(%1)\n2:"
               ::"s"(st.mode), "n"(W_NORM - 1), "n"(W_SLOW - 1) : "scc", "memory");
  VMWAIT(W_FAST - 1);
  st.buf[id_at(I)] = issue<id_at(I)>(S);
  SB();
  if constexpr ((I & 1) == 0 && I <= PACE_END) {
    st.mode = __builtin_amdgcn_readfirstlane(st.mnext);
    *(volatile LAS unsigned*)(L + L_PROG + wave * 4) = I;
    st.mnext = *(volatile LAS unsigned*)(L + L_MODE + wave * 4);
  }
}
template <int I, int HI> __device__ __forceinline__ void fill(State& st, const Stream& S, LAS unsigned char* L, int wave) {
  if constexpr (I < HI) {
    step<I>(st, S, L, wave);
    SB();
    fill<I + 1, HI>(st, S, L, wave);
  }
}
constexpr int cmin(int a, int b) { return a < b ? a : b; }
template <int I, int P> __device__ __forceinline__ void mainloop(State& st, const Stream& S, LAS unsigned char* L, int wave, int lane) {
  if constexpr (I < 80) {
    step<I>(st, S, L, wave);
    constexpr int PH = cmin(P + 4, I + 1 - W_FAST);
    proc_range<P, PH>(st, L, wave, lane);
    SB();
    mainloop<I + 1, PH>(st, S, L, wave, lane);
  } else if constexpr (P < 80) {
    if constexpr (P == 78) { VMWAIT(1); st.buf[80] = issue<80>(S); SB(); }
    proc_range<P, P + 1>(st, L, wave, lane);
    SB();
    mainloop<I, P + 1>(st, S, L, wave, lane);
  }
}

__global__ void __launch_bounds__(NTHR, 3) fused_decode(const void*, const void*, const void*, const void*, const void*, const void*,
                                                        void*, void*, const void*, const void*, const void*, const void*) {
  v8p pa; v2p pb;
  asm volatile("s_load_dwordx16 %0, %2, 0x0\n\ts_load_dwordx4 %1, %2, %3" : "=&s"(pa), "=&s"(pb)
               : "s"(__builtin_amdgcn_kernarg_segment_ptr()), "s"(64 + (blockIdx.x & 4) * 4) : "memory");
  SB();
  __shared__ __attribute__((aligned(16))) unsigned char smem[LDS_BYTES];
  LAS unsigned char* L = (LAS unsigned char*)smem;
  const int tid = threadIdx.x, lane = tid & 63, wave = __builtin_amdgcn_readfirstlane(tid >> 6);
  unsigned cp0 = 0, cp1 = 0, cp2 = 0;
  if (wave == 0) {
    unsigned long long pc; asm volatile("s_getpc_b64 %0" : "=s"(pc));
    const rsrc_t rc = mk((const void*)(pc & ~127ull), CODE_BYTES);
    cp0 = __builtin_amdgcn_raw_buffer_load_b32(rc, lane * 128, 0, 0); cp1 = __builtin_amdgcn_raw_buffer_load_b32(rc, lane * 128, 8192, 0);
    cp2 = __builtin_amdgcn_raw_buffer_load_b32(rc, lane * 128, 16384, 0);
  }
  SB();
  const int bid = blockIdx.x, w = (bid & 7) * 32 + (bid >> 3);
  const int pair = w >> 3, s = w & 7, b = pair >> 4, kvh = pair & 15;
  const int g = lane >> 4, n = lane & 15, j0 = n & 7;
  const int v0 = 9 * (w & 31), v = v0 + wave, urb = (w & 96) + (v & 31);
  unsigned offa = (pair * SK + SK - WIN) * (DH * 2) + ((s & 3) << 15) + ((s >> 2) << 18), offb = pair * SK * (DH * 2) + ((s & 1) << 15) + ((s >> 1) << 18);
  unsigned offw = urb * (32 * DM * 2) + (v >> 5) * 128, offq = (s & 1) * (288 * 8192) + kvh * 512 + (s >> 1) * 128;
  int qvo = (wave * 32 + g * 8 + (n >> 3) * 4) * 8192 + j0 * 16, xvo = ((n >> 3) * DM + (s & 1) * 288 + wave * 32 + g * 8) * 2;
  unsigned tk = (pair << 21) + 0x180000 + pair * 4352, tw0 = (w & 96) * (32 * DM * 2), tw1 = tw0 + 32 * (32 * DM * 2) - 128;
  PINS(offa); PINS(offb); PINS(offw); PINS(offq); PINV(qvo); PINV(xvo); PINS(tk); PINS(tw0); PINS(tw1);
  unsigned long long t64;
  SB();
  asm volatile("s_memrealtime %2\n\ts_waitcnt lgkmcnt(0)" : "+s"(pa), "+s"(pb), "=&s"(t64)::"memory");
  SB();
  const unsigned t_start = (unsigned)t64;
  typedef const char* P;
  const P x = (P)pa[0], kA = (P)pa[1], vA = (P)pa[2], kB = (P)pa[3], vB = (P)pa[4], gw = (P)pa[5], ws = (P)pa[7], wqb = (P)pb[0], wob = (P)pb[1];
  unsigned e0, d1, d2, d3, d4;
  asm volatile("s_cmp_lg_u32 %16, 9\n\ts_cbranch_scc1 1f\n\t"
               "s_cmp_lg_u32 %17, 0\n\ts_cbranch_scc1 2f\n\t"
               "s_load_dword %1, %6, %13\n\ts_load_dword %2, %7, %13\n\ts_load_dword %3, %8, %13\n\ts_load_dword %4, %9, %13\n"
               "2:\n\ts_cmp_lg_u32 %17, 1\n\ts_cbranch_scc1 3f\n\t"
               "s_load_dword %1, %10, 0x0\n\ts_load_dword %2, %12, %14\n\ts_load_dword %3, %12, %15\n\ts_load_dword %4, %11, 0x0\n"
               "3:\n\ts_load_dword %0, %5, 0x0\n\ts_barrier\n1:"
               : "=&s"(e0), "=&s"(d1), "=&s"(d2), "=&s"(d3), "=&s"(d4)
               : "s"(ws), "s"(kA), "s"(vA), "s"(kB), "s"(vB), "s"(x), "s"(gw), "s"(wob), "s"(tk), "s"(tw0), "s"(tw1), "s"(wave), "s"(s)
               : "scc", "memory");

  if (wave == 9) {
    asm volatile("s_waitcnt lgkmcnt(0)" : "+s"(e0), "+s"(d1), "+s"(d2), "+s"(d3), "+s"(d4)::"memory");
    const rsrc_t rws = mk(ws, WS_END);
    const unsigned tg = (e0 + 1u) & 0xffffu;
    volatile LAS unsigned* prog = (volatile LAS unsigned*)(L + L_PROG) + j0;
    volatile LAS unsigned* modes = (volatile LAS unsigned*)(L + L_MODE) + j0;
    int ref = 0; unsigned tref = 0; bool have = false;
    unsigned until = ((t_start >> 8) + 1) << 8;
#pragma nounroll
    for (unsigned spins = 0; spins < SPIN_CAP; ++spins) {
      const int p = *prog;
      if (__any(p >= PACE_END)) break;
      const unsigned now = now32();
      if (have) {
        const int lead = (short)(p * (2 * TAU) - 2 * now - ref);
        *modes = lead > LEAD_HI ? 2u : (lead < -LEAD_LO ? 0u : 1u);
      }
      if ((int)(now - until) >= 0) {
        const int slot = WS_PACE + ((until >> 8) & 31) * 1024;
        if (until & 128) {
          MEMFENCE();
          const unsigned o = __builtin_amdgcn_raw_buffer_load_b32(rws, j0 * 128, slot, AUX_SC1);
          if (__all((o & 0xffffu) == tg)) {
            const int b0 = __builtin_amdgcn_readfirstlane(o >> 16);
            int d = (short)((o >> 16) - b0);
            d += dppu<XOR1>(d); d += dppu<XOR2>(d); d += dppu<XOR7>(d);
            ref = b0 + (d >> 3); tref = until - 128; have = true;
          }
        } else if ((bid >> 3) == PUB) {
          int sum = p + dppu<XOR1>(p); sum += dppu<XOR2>(sum); sum += dppu<XOR7>(sum);
          __builtin_amdgcn_raw_buffer_store_b32((((sum * TAU - 8 * now) >> 2) << 16) | tg, rws, lane ? OOB : (bid & 7) * 128, slot, AUX_SC1);
        }
        until += 128;
      } else __builtin_amdgcn_s_sleep(4);
    }
    const int rem = (short)(80 * 2 * TAU - __builtin_amdgcn_readfirstlane(ref) - 2 * tref);
    const unsigned due = have ? tref + ((rem * 243) >> 9) + DUE_OB : now32();
    *(volatile LAS unsigned*)(L + L_FLAG + 20) = due;
    wg_barrier();
    if (b) {
      const unsigned te = now32();
#pragma nounroll
      for (unsigned spins = 0; spins < 64; ++spins) {
        const unsigned now = now32();
        if ((int)(now - due) >= 0 && (int)(now - te) >= DUE_OWN) break;
        __builtin_amdgcn_s_sleep(1);
      }
    }
    wg_barrier();
    ENDWAVE();
  }

  const rsrc_t rwq = mk(wqb + offq, 287 * 8192 + 128);
  v4u wq[4];
#pragma unroll
  for (int i = 0; i < 4; ++i) wq[i] = ld128<AUX_NT>(rwq, qvo, i * 8192);
  const v4u xr = ld128<0>(mk(x, 2 * DM * 2), xvo, 0);
  SB();
  const P kap = kA + offa, wop = wob + offw;
  constexpr int A_SPAN = aoff(7) + 7 * 4096 + 1024, B_SPAN = boff(31) + 7 * 4096 + 1024, WO_SPAN = 31 * 1152 + 128;
  Stream S;
  S.kvo = wave * 4096 + (n >> 2) * 256 + g * 64 + (n & 3) * 16;
  S.ovo = (lane >> 3) * 1152 + j0 * 16;
  State st;
  if (tid >= 512 && tid < 536) *(LAS unsigned*)(L + L_FLAG + lane * 4) = lane < 16 ? 0u : 1u;
  {
    const rsrc_t r0 = mk(wave < 8 ? kap : wop, wave < 8 ? A_SPAN : WO_SPAN);
    const int vo0 = wave < 8 ? S.kvo : S.ovo, st0 = wave < 8 ? 1024 : 8 * 1152;
    SB(); wg_barrier(); SB();
#pragma unroll
    for (int i = 0; i < NB0; ++i) st.buf[i] = ld128<AUX_NT>(r0, vo0, i * st0);
  }
  SB();
  const rsrc_t rws = mk(ws, WS_END);
  S.ka = mk(kap, A_SPAN); S.va = mk(vA + offa, A_SPAN);
  S.kb = mk(kB + offb, B_SPAN); S.vb = mk(vB + offb, B_SPAN);
  S.wo = mk(wop, WO_SPAN);
  S.vvo = wave * 4096 + lane * 16;
#pragma unroll
  for (int i = 0; i < 4; ++i) st.msk[i] = ((j0 >> 1) == i) ? ((j0 & 1) ? 0xffff0000u : 0x0000ffffu) : 0u;
  st.p1 = lane & 1; st.p2 = lane & 2;
  {
    f32x4 d0 = {0.f, 0.f, 0.f, 0.f}, d1 = {0.f, 0.f, 0.f, 0.f};
#pragma unroll
    for (int i = 0; i < 4; ++i) {
      const unsigned sel = (i & 1) ? 0x03020302u : 0x01000100u;
      d0 = mfma(sparse(perm(xr[i >> 1], xr[i >> 1], sel), st.msk), wq[i], d0);
      d1 = mfma(sparse(perm(xr[2 + (i >> 1)], xr[2 + (i >> 1)], sel), st.msk), wq[i], d1);
    }
    f32x4 y;
#pragma unroll
    for (int i = 0; i < 4; ++i) y[i] = d0[i] + dpp<ROR8>(d1[i]);
    *(LAS f32x4*)(L + L_QP + (n < 8 ? wave : 9) * 512 + (g >> 1) * 256 + j0 * 32 + (g & 1) * 16) = y;
    lds_count<0>(L);
    asm volatile("" ::"v"(cp0), "v"(cp1), "v"(cp2));
    SB();
  }
  unsigned ep, T, T16;
  auto wo_frags = [&](const v4u w0, const v4u w1, const v4u w2, const v4u w3, v4u (&bf)[4]) INL {
    LAS unsigned char* wr = L + L_WO + wave * 4608 + (lane >> 3) * 144 + j0 * 16;
    *(LAS v4u*)(wr) = w0; *(LAS v4u*)(wr + 8 * 144) = w1; *(LAS v4u*)(wr + 16 * 144) = w2; *(LAS v4u*)(wr + 24 * 144) = w3;
    MEMFENCE();
    LAS unsigned char* rd = L + L_WO + wave * 4608 + (g * 8 + (n >> 2)) * 144 + (n & 3) * 8;
#pragma unroll
    for (int t = 0; t < 4; ++t) {
      const v2u lo = ldtr(rd + t * 32), hi = ldtr(rd + 4 * 144 + t * 32);
      bf[t] = v4u{lo[0], lo[1], hi[0], hi[1]};
    }
    SB();
  };
  int ovo = ((urb >> 3) * 8 + (lane >> 3)) * MBS + (b ? WS_OB + (urb & 7) * 128 + j0 * 16 : WS_OA + (urb & 7) * 64 + (j0 >> 1) * 16);
  unsigned psel = b ? 0x07060302u : (j0 & 1) ? 0x07060504u : 0x03020100u, tmask = b ? 0xffffu : 0u;
  int awa = L_A + wave * 1280 + (lane >> 3) * 80 + j0 * 8, ara = L_A + wave * 1280 + n * 80 + g * 16;
  int wra = L_W + (((v >> 3) - (v0 >> 3)) * 2 + ((urb >> 2) & 1)) * 64 + g * 16;
  const int cut = 32 - (v0 & 31);
  int pwa = L_PP + (wave >= cut ? 4608 : 0) + wave * 512 + (lane >> 5) * 256 + (lane & 31) * 4;
  auto gather = [&](v4u& o0, v4u& o1) INL { o0 = ld128<AUX_SC1>(rws, ovo, 0); o1 = ld128<AUX_SC1>(rws, ovo, 128 * MBS); };
  auto project = [&](const v4u (&bf)[4], v4u o0, v4u o1) INL {
    const f32x4 wt = *(LAS f32x4*)(L + wra);
    SB();
    v4u a;
    auto take = [&]() INL -> bool {
      *(LAS v2u*)(L + awa) = v2u{perm(o0[2], o0[0], psel), perm(o0[3], o0[1], psel)};
      *(LAS v2u*)(L + awa + 640) = v2u{perm(o1[2], o1[0], psel), perm(o1[3], o1[1], psel)};
      MEMFENCE();
      a = *(LAS v4u*)(L + ara);
      SB();
      const unsigned b0 = __builtin_amdgcn_bitop3_b32(o0[0], o0[1], T16, 0x7e), b1 = __builtin_amdgcn_bitop3_b32(o0[2], o0[3], T16, 0x7e);
      const unsigned b2 = __builtin_amdgcn_bitop3_b32(o1[0], o1[1], T16, 0x7e), b3 = __builtin_amdgcn_bitop3_b32(o1[2], o1[3], T16, 0x7e);
      return __all(((b0 | b1 | b2 | b3) & tmask) == 0);
    };
    unsigned spins = SPIN_CAP;
    bool ok = take();
#pragma nounroll
    while (!ok) {
      __builtin_amdgcn_s_sleep(1);
      MEMFENCE();
      gather(o0, o1);
      ok = take() | (--spins == 0);
    }
    f32x4 d[4];
#pragma unroll
    for (int t = 0; t < 4; ++t) d[t] = mfma(a, bf[t], f32x4{0.f, 0.f, 0.f, 0.f});
    SB();
    float sm[4];
#pragma unroll
    for (int t = 0; t < 4; ++t) { const f32x2 h = f32x2{wt[0], wt[1]} * f32x2{d[t][0], d[t][1]} + f32x2{wt[2], wt[3]} * f32x2{d[t][2], d[t][3]}; sm[t] = h[0] + h[1]; }
    *(LAS float*)(L + pwa) = t16_sum(sm[0], sm[1]);
    *(LAS float*)(L + pwa + 128) = t16_sum(sm[2], sm[3]);
  };
  auto publish_p = [&](const int pra, const int pxo) INL {
    f32x2 p[9];
#pragma unroll
    for (int j = 0; j < 9; ++j) p[j] = *(LAS f32x2*)(L + pra + j * 512);
    const f32x2 y = ((p[0] + p[1]) + (p[2] + p[3])) + ((p[4] + p[5]) + (p[6] + p[7] + p[8]));
    __builtin_amdgcn_raw_buffer_store_b64(v2u{tag8(y[0], T), tag8(y[1], T)}, rws, pxo, 0, AUX_SC1);
  };

  if (__builtin_expect(wave < 8, 1)) {
    st.mode = 1; st.mnext = 1;
    fill<NB0, N_FILL>(st, S, L, wave);
    lds_wait_ge(L, 1, 1u);
    ep = __builtin_amdgcn_readfirstlane(*(LAS unsigned*)(L + L_FLAG + 16)); T = ep & 0xffu; T16 = ep & 0xffffu;
    st.q[0] = *(LAS v4u*)(L + L_Q + (n >> 3) * 256 + (g * 4 + (n & 3)) * 16);
    st.q[1] = *(LAS v4u*)(L + L_Q + 512 + (n >> 3) * 256 + (g * 4 + (n & 3)) * 16);
    st.acc[0] = f32x4{0.f, 0.f, 0.f, 0.f}; st.acc[1] = f32x4{0.f, 0.f, 0.f, 0.f};
    int pra = L_PP + 4608 + lane * 8, pxo = WS_PX + w * MBS + 512 + lane * 8;
    PINV(ovo); PINV(pwa); PINV(T16); PINV(awa); PINV(ara); PINV(wra); PINV(psel); PINV(tmask); PINV(pra); PINV(pxo);
    mainloop<N_FILL, 0>(st, S, L, wave, lane);
    *(LAS f32x4*)(L + L_OP + 8192 + wave * 1024 + lane * 16) = st.acc[1];
    SB();
    st.buf[81] = issue<81>(S); st.buf[82] = issue<82>(S); st.buf[83] = issue<83>(S);
    SB();
    wg_barrier();
    wg_barrier();
    v4u bf[4], o0, o1;
    gather(o0, o1);
    SB();
    wo_frags(st.buf[80], st.buf[81], st.buf[82], st.buf[83], bf);
    project(bf, o0, o1);
    wg_barrier();
    if (wave == 0 && cut < 9) publish_p(pra, pxo);
    return;
  }

  __builtin_amdgcn_s_setprio(3);
  static_assert(NB0 == 2, "the rest of the control wave's wo tile");
  st.buf[2] = issue<82>(S); st.buf[3] = issue<83>(S);
  ep = *(const CAS unsigned*)ws + 1u;
  PINS(ep);
  T = ep & 0xffu; T16 = ep & 0xffffu;
  *(LAS unsigned*)(L + L_FLAG + 16) = ep;
  v4u bf[4];
  lds_wait_ge(L, 0, 9u);
  {
    f32x4 qs = *(LAS f32x4*)(L + L_QP + (lane & 31) * 16);
#pragma unroll
    for (int i = 1; i < 9; ++i) qs += *(LAS f32x4*)(L + L_QP + i * 512 + (lane & 31) * 16);
    qs = __builtin_bit_cast(f32x4, __builtin_bit_cast(v4u, qs) + 0x80u);
    st128(v4u{tag8(qs[0], T), tag8(qs[1], T), tag8(qs[2], T), tag8(qs[3], T)}, rws, lane < 32 ? lane * 16 : OOB, WS_QX + w * MBS);
  }
  wo_frags(st.buf[0], st.buf[1], st.buf[2], st.buf[3], bf);
  PINV(bf[0]); PINV(bf[1]); PINV(bf[2]); PINV(bf[3]);
#pragma nounroll
  for (unsigned spins = 0; spins < 4096 && (int)(now32() - t_start) < DLY_Q; ++spins) __builtin_amdgcn_s_sleep(2);
  {
    v4u qx[4];
    const int gvo = g * 2 * MBS + n * 16, gso = WS_QX + kvh * 8 * MBS + b * 256;
#pragma nounroll
    for (unsigned spins = 0; spins < SPIN_CAP; ++spins) {
      MEMFENCE();
#pragma unroll
      for (int i = 0; i < 4; ++i) qx[i] = ld128<AUX_SC1>(rws, gvo, gso + ((i >> 1) * 128 + (i & 1)) * MBS) ^ T;
      const v4u bad = qx[0] | qx[1] | qx[2] | qx[3];
      if (__all(((bad[0] | bad[1] | bad[2] | bad[3]) & 0xffu) == 0)) break;
      __builtin_amdgcn_s_sleep(16);
    }
#pragma unroll
    for (int br = 0; br < 2; ++br) {
      const f32x4 v = __builtin_bit_cast(f32x4, qx[br * 2]) + __builtin_bit_cast(f32x4, qx[br * 2 + 1]);
      *(LAS v2u*)(L + L_Q + br * 512 + lane * 8) = v2u{cvtpk(v[0], v[1]), cvtpk(v[2], v[3])};
    }
    lds_count<1>(L);
  }
  float gt = 0.f;
  int rvo[5] = {OOB, OOB, OOB, OOB, OOB};
  if (w < 72) {
    const rsrc_t rg = mk(gw, DM * 2), rxr = mk(x + ((w >> 2) & 1) * (DM * 2), DM * 2);
    float z = 0.f;
#pragma unroll
    for (int i = 0; i < 5; ++i) {
      const unsigned gg = __builtin_amdgcn_raw_buffer_load_b32(rg, (i * 64 + lane) * 4, 0, 0), xx = __builtin_amdgcn_raw_buffer_load_b32(rxr, (i * 64 + lane) * 4, 0, 0);
      z += rbf(u2f(xx << 16) * u2f(gg << 16)) + rbf(u2f(xx & 0xffff0000u) * u2f(gg & 0xffff0000u));
    }
    z = x32_sum(z); z = x16_sum(z); z += dpp<ROR8>(z); z += dpp<ROR4>(z); z += dpp<XOR2>(z); z += dpp<XOR1>(z);
    gt = rbf(1.f / (1.f + __expf(-rbf(z))));
    gt = (lane & 32) ? rbf(1.f - gt) : gt;
#pragma unroll
    for (int i = 0; i < 5; ++i) {
      const int c = w >> 3, j = (32 * c) / 9 + i;
      rvo[i] = 9 * j <= 32 * c + 31 ? WS_PX + ((lane >> 3) * 32 + j) * MBS + (((9 * j) >> 5) == c ? 0 : 512) + (w & 7) * 64 + j0 * 8 : OOB;
    }
  }
  unsigned tv = T, t4 = rvo[4] == OOB ? 0u : T;
#pragma unroll
  for (int i = 0; i < 5; ++i) PINV(rvo[i]);
  PINV(tv); PINV(t4);
  PINV(gt);
  lds_wait_ge(L, 2, 8u);
  v2u mlw;
  {
    const v2u ml = *(LAS v2u*)(L + L_ML + (lane & 31) * 8);
    const float m = u2f(ml[0]);
    float M = fmaxf(m, dpp<XOR1>(m)); M = fmaxf(M, dpp<XOR2>(M)); M = fmaxf(M, dpp<XOR7>(M));
    const float e = ex2(m - M);
    float Ls = e * u2f(ml[1]);
    PINV(Ls);
    Ls += dpp<XOR1>(Ls); Ls += dpp<XOR2>(Ls); Ls += dpp<XOR7>(Ls);
    Ls = u2f(f2u(Ls) & 0xffffff00u);
    *(LAS float*)(L + L_F + (lane & 31) * 4) = e / Ls;
    mlw = v2u{(f2u(M) & 0xffff0000u) | T16, f2u(Ls) | T};
    MEMFENCE();
  }
  auto combine = [&](const int br, const f32x4 f0, const f32x4 f1) INL -> f32x4 {
    f32x4 op[8];
#pragma unroll
    for (int i = 0; i < 8; ++i) op[i] = *(LAS f32x4*)(L + L_OP + br * 8192 + i * 1024 + lane * 16);
    f32x4 O = f0[0] * op[0];
    O += f0[1] * op[1]; O += f0[2] * op[2]; O += f0[3] * op[3];
    O += f1[0] * op[4]; O += f1[1] * op[5]; O += f1[2] * op[6]; O += f1[3] * op[7];
    return O;
  };
  const int dpos = (g >> 1) * 128 + n * 8 + (g & 1) * 4, fa = L_F + (g >> 1) * 32;
  lds_wait_ge(L, 3, 8u);
  {
    const f32x4 O = combine(0, *(LAS f32x4*)(L + fa), *(LAS f32x4*)(L + fa + 16));
    __builtin_amdgcn_raw_buffer_store_b64(v2u{cvtpk(O[0], O[1]), cvtpk(O[2], O[3])}, rws, dpos * 2, WS_OA + w * MBS, AUX_SC1);
    asm volatile("s_waitcnt vmcnt(0)" ::: "memory");
    __builtin_amdgcn_raw_buffer_store_b64(mlw, rws, (lane & 0x27) ? OOB : lane, WS_ML + w * MBS, AUX_SC1);
  }
  f32x4 fb0 = *(LAS f32x4*)(L + fa + 64), fb1 = *(LAS f32x4*)(L + fa + 80);
  if (cut < 9) {
#pragma unroll
    for (int j = 0; j < 9; ++j) *(LAS v4u*)(L + L_PP + j * 1024 + lane * 16) = v4u{0u, 0u, 0u, 0u};
  }
  int svo = WS_ML + ((g & 1) * 128 + (w & 96) + ((v0 + (lane >> 5) * 8) & 24) + ((lane >> 1) & 7)) * MBS + b * 16 + (lane & 1) * 8;
  int wwa = L_W + (((lane >> 5) * 2 + (lane & 1)) * 16 + ((lane >> 1) & 15)) * 4;
  int obo = WS_OB + w * MBS + dpos * 4, pxo = WS_PX + w * MBS + lane * 8, pra = L_PP + lane * 8;
  GAS unsigned short* op = (GAS unsigned short*)pa[6] + ((w >> 2) & 1) * DM + (w >> 3) * 64 + (w & 3) * 16 + j0 * 2 + (g & 1);
  unsigned sl = 0x05040100u, sh = 0x07060100u;
  PINV(awa); PINV(ara); PINV(wra); PINV(psel); PINV(tmask); PINV(ovo); PINV(pwa); PINV(T16);
  PINV(fb0); PINV(fb1); PINV(obo); PINV(pxo); PINV(pra); PINV(op); PINV(svo); PINV(wwa); PINV(sl); PINV(sh);
  lds_wait_ge(L, L_PROG / 4, STAT_AT);
  v2u sx = __builtin_amdgcn_raw_buffer_load_b64(rws, svo, 0, AUX_SC1);
  {
    sx ^= v2u{T16, T};
    unsigned spins = SPIN_CAP;
    bool ok = __all(((sx[0] & 0xffffu) | (sx[1] & 0xffu)) == 0);
#pragma nounroll
    while (!ok) {
      __builtin_amdgcn_s_sleep(8);
      MEMFENCE();
      sx = __builtin_amdgcn_raw_buffer_load_b64(rws, svo, 0, AUX_SC1) ^ v2u{T16, T};
      ok = __all(((sx[0] & 0xffffu) | (sx[1] & 0xffu)) == 0) | (--spins == 0);
    }
    const float m = u2f(sx[0]);
    float M = fmaxf(m, dpp<XOR2>(m)); M = fmaxf(M, dpp<ROR4>(M)); M = fmaxf(M, dpp<ROR8>(M));
    float wg = ex2(m - M) * u2f(sx[1]);
    PINV(wg);
    float Lg = wg + dpp<XOR2>(wg); Lg += dpp<ROR8>(Lg); Lg += dpp<ROR4>(Lg);
    *(LAS float*)(L + wwa) = wg * __builtin_amdgcn_rcpf(Lg);
  }
  VMWAIT(0);
  wg_barrier();
  {
    const f32x4 O = combine(1, fb0, fb1);
    const unsigned c0 = cvtpk(O[0], O[1]), c1 = cvtpk(O[2], O[3]);
    st128(v4u{perm(c0, T16, sl), perm(c1, T16, sl), perm(c0, T16, sh), perm(c1, T16, sh)}, rws, obo, 0);
  }
  wg_barrier();
  { v4u o0, o1; gather(o0, o1); SB(); project(bf, o0, o1); }
  wg_barrier();
  publish_p(pra, pxo);
  if (w >= 72) ENDWAVE();
  {
    v2u px[5], a0 = {0u, 0u}, a1 = a0, a2 = a0, a3 = a0, a4 = a0, b0 = a0, b1 = a0, b2 = a0, b3 = a0, b4 = a0;
    const v4u rd = {(unsigned)(size_t)ws, (unsigned)((size_t)ws >> 32) & 0xffffu, (unsigned)WS_END, 0x00020000u};
#define POLL5(r0, r1, r2, r3, r4) asm volatile("v_mov_b64 %0, 0\n\tv_mov_b64 %1, 0\n\tv_mov_b64 %2, 0\n\tv_mov_b64 %3, 0\n\tv_mov_b64 %4, 0\n\t" \
      "buffer_atomic_add_x2 %0, %5, %10, 0 offen sc0 sc1\n\tbuffer_atomic_add_x2 %1, %6, %10, 0 offen sc0 sc1\n\t" \
      "buffer_atomic_add_x2 %2, %7, %10, 0 offen sc0 sc1\n\tbuffer_atomic_add_x2 %3, %8, %10, 0 offen sc0 sc1\n\t" \
      "buffer_atomic_add_x2 %4, %9, %10, 0 offen sc0 sc1" \
      : "+v"(r0), "+v"(r1), "+v"(r2), "+v"(r3), "+v"(r4) : "v"(rvo[0]), "v"(rvo[1]), "v"(rvo[2]), "v"(rvo[3]), "v"(rvo[4]), "s"(rd) : "memory")
#define TAKE5(r0, r1, r2, r3, r4) asm volatile("s_waitcnt vmcnt(5)" : "+v"(r0), "+v"(r1), "+v"(r2), "+v"(r3), "+v"(r4)::"memory"); \
      px[0] = r0 ^ tv; px[1] = r1 ^ tv; px[2] = r2 ^ tv; px[3] = r3 ^ tv; px[4] = r4 ^ t4; \
      if (__all(((px[0][0] | px[0][1] | px[1][0] | px[1][1] | px[2][0] | px[2][1] | px[3][0] | px[3][1] | px[4][0] | px[4][1]) & 0xffu) == 0)) break
    const unsigned due = __builtin_amdgcn_readfirstlane(*(volatile LAS unsigned*)(L + L_FLAG + 20)) + DUE_PX;
#pragma nounroll
    for (unsigned spins = 0; spins < 64 && (int)(now32() - due) < 0; ++spins) __builtin_amdgcn_s_sleep(1);
    POLL5(a0, a1, a2, a3, a4);
    __builtin_amdgcn_s_sleep(6);
#pragma nounroll
    for (unsigned spins = 0; spins < SPIN_CAP; ++spins) {
      POLL5(b0, b1, b2, b3, b4);
      TAKE5(a0, a1, a2, a3, a4);
      POLL5(a0, a1, a2, a3, a4);
      TAKE5(b0, b1, b2, b3, b4);
    }
#undef POLL5
#undef TAKE5
    const f32x2 y = (__builtin_bit_cast(f32x2, px[0]) + __builtin_bit_cast(f32x2, px[1])) + (__builtin_bit_cast(f32x2, px[2]) + __builtin_bit_cast(f32x2, px[3])) + __builtin_bit_cast(f32x2, px[4]);
    float ev = t16_sum(y[0], y[1]);
    ev += dpp<ROR8>(ev);
    const float r = x32_sum(rbf(gt * rbf(ev)));
    *op = (unsigned short)(cvtpk(r, r) & 0xffffu);
    if (w == 0) __builtin_amdgcn_raw_buffer_store_b32(ep, rws, lane ? OOB : 0, 0, AUX_SC1);
    asm volatile("s_waitcnt vmcnt(0)" : "+v"(a0), "+v"(a1), "+v"(a2), "+v"(a3), "+v"(a4), "+v"(b0), "+v"(b1), "+v"(b2), "+v"(b3), "+v"(b4)::"memory");
  }
  ENDWAVE();
}

extern "C" void kernel_launch(void* const* d_in, const int* in_sizes, int n_in, void* d_out, int out_size, void* d_ws, size_t ws_size,
                              hipStream_t stream) {
  fused_decode<<<256, NTHR, 0, stream>>>(d_in[0], d_in[1], d_in[2], d_in[6], d_in[7], d_in[10], d_out, d_ws, d_in[3], d_in[4], d_in[8], d_in[9]);
}
```

```cpp
#include <hip/hip_runtime.h>
#include <stdint.h>

#define GAS __attribute__((address_space(1)))
#define LAS __attribute__((address_space(3)))
typedef unsigned v4u __attribute__((ext_vector_type(4)));
typedef unsigned v2u __attribute__((ext_vector_type(2)));
typedef float f32x4 __attribute__((ext_vector_type(4)));
typedef float f32x2 __attribute__((ext_vector_type(2)));
typedef unsigned long long v8p __attribute__((ext_vector_type(8)));
typedef unsigned long long v2p __attribute__((ext_vector_type(2)));
typedef short bf16x8 __attribute__((ext_vector_type(8)));
typedef short v4s __attribute__((ext_vector_type(4)));
typedef __bf16 bf16x2 __attribute__((ext_vector_type(2)));
typedef __amdgpu_buffer_rsrc_t rsrc_t;

constexpr int DM = 576, SK = 8192, DH = 128, WIN = 2048;
constexpr int NTHR = 640;
constexpr int AUX_NT = 2, AUX_SC1 = 16;
constexpr unsigned SPIN_CAP = 1u << 18;
constexpr float LOG2E = 1.4426950408889634f;
constexpr int W_FAST = 4, W_NORM = 3, W_SLOW = 2;
constexpr int LEAD_HI = 70, LEAD_LO = 120;
constexpr int TAU = 29;
constexpr int PUB = 21;
constexpr int NB0 = 2;
constexpr int N_FILL = 24;
constexpr int PACE_END = 76;
constexpr int STAT_AT = 66;
constexpr int DUE_OB = 142, DUE_OWN = 40;
constexpr int DUE_PX = 85;
constexpr int DLY_Q = 500;
constexpr int CODE_BYTES = 17920;
constexpr int WS_PACE = 4096;
constexpr int WS_MB = WS_PACE + 32768, MBS = 3200;
constexpr int WS_QX = WS_MB;
constexpr int WS_OA = WS_MB + 512;
constexpr int WS_OB = WS_MB + 1024;
constexpr int WS_PX = WS_MB + 2048;
constexpr int WS_ML = WS_MB + 3072;
constexpr int WS_END = WS_MB + 256 * MBS;
constexpr int L_FLAG = 0;
constexpr int L_PROG = 32, L_MODE = 64;
constexpr int L_ML = 128;
constexpr int L_F = L_ML + 256;
constexpr int L_W = L_F + 128;
constexpr int L_Q = L_W + 256;
constexpr int L_QP = L_Q + 1024;
constexpr int L_PP = L_QP + 10 * 512;
constexpr int L_OP = L_PP + 2 * 4608;
constexpr int L_WO = L_OP + 16384;
constexpr int L_A = L_WO + 9 * 4608;
constexpr int L_END = L_A + 9 * 1280;
constexpr int LDS_BYTES = 88 * 1024;
static_assert(L_END <= LDS_BYTES, "lds");

#define VMWAIT(n) __builtin_amdgcn_s_waitcnt(((n) & 15) | (((n) >> 4) << 14) | 0x0F70)
#define LGKM0() asm volatile("s_waitcnt lgkmcnt(0)" ::: "memory")
#define SB() __builtin_amdgcn_sched_barrier(0)
#define PINV(x) asm volatile("" : "+v"(x))
#define PINS(x) asm volatile("" : "+s"(x))
#define MEMFENCE() asm volatile("" ::: "memory")
#define ENDWAVE() do { __builtin_amdgcn_endpgm(); __builtin_unreachable(); } while (0)
#define INL __attribute__((always_inline))
constexpr int OOB = 0x40000000;

__device__ __forceinline__ rsrc_t mk(const void* p, int bytes) { return __builtin_amdgcn_make_buffer_rsrc((void*)p, 0, bytes, 0x00020000); }
template <int AUX> __device__ __forceinline__ v4u ld128(rsrc_t r, int vo, int so) { return __builtin_amdgcn_raw_buffer_load_b128(r, vo, so, AUX); }
__device__ __forceinline__ void st128(v4u v, rsrc_t r, int vo, int so) {
  __builtin_amdgcn_raw_buffer_store_b128(v, r, vo, so, AUX_SC1);
  SB(); asm volatile("s_nop 1"); SB();
}
__device__ __forceinline__ f32x4 mfma(v4u a, v4u b, f32x4 c) {
  return __builtin_amdgcn_mfma_f32_16x16x32_bf16(__builtin_bit_cast(bf16x8, a), __builtin_bit_cast(bf16x8, b), c, 0, 0, 0);
}
__device__ __forceinline__ v2u ldtr(LAS unsigned char* p) { return __builtin_bit_cast(v2u, __builtin_amdgcn_ds_read_tr16_b64_v4i16((LAS v4s*)p)); }
__device__ __forceinline__ float u2f(unsigned u) { return __builtin_bit_cast(float, u); }
__device__ __forceinline__ unsigned f2u(float f) { return __builtin_bit_cast(unsigned, f); }
__device__ __forceinline__ unsigned perm(unsigned hi, unsigned lo, unsigned sel) { return __builtin_amdgcn_perm(hi, lo, sel); }
__device__ __forceinline__ unsigned cvtpk(float lo, float hi) {
  bf16x2 r; r.x = (__bf16)lo; r.y = (__bf16)hi; return __builtin_bit_cast(unsigned, r);
}
__device__ __forceinline__ float rbf(float x) { return u2f(cvtpk(x, x) & 0xffff0000u); }
__device__ __forceinline__ float ex2(float x) { return __builtin_amdgcn_exp2f(x * LOG2E); }
template <int CTRL> __device__ __forceinline__ unsigned dppu(unsigned x) { return (unsigned)__builtin_amdgcn_mov_dpp((int)x, CTRL, 0xf, 0xf, true); }
template <int CTRL> __device__ __forceinline__ float dpp(float x) { return u2f(dppu<CTRL>(f2u(x))); }
constexpr int XOR1 = 0xB1, XOR2 = 0x4E, XOR3 = 0x1B, XOR7 = 0x141, ROR8 = 0x128, ROR4 = 0x124;
__device__ __forceinline__ float x16_max(float x) { auto s = __builtin_amdgcn_permlane16_swap(f2u(x), f2u(x), false, false); return fmaxf(u2f(s[0]), u2f(s[1])); }
__device__ __forceinline__ float x32_max(float x) { auto s = __builtin_amdgcn_permlane32_swap(f2u(x), f2u(x), false, false); return fmaxf(u2f(s[0]), u2f(s[1])); }
__device__ __forceinline__ float t16_sum(float a, float b) { auto s = __builtin_amdgcn_permlane16_swap(f2u(a), f2u(b), false, false); return u2f(s[0]) + u2f(s[1]); }
__device__ __forceinline__ float t32_sum(float a, float b) { auto s = __builtin_amdgcn_permlane32_swap(f2u(a), f2u(b), false, false); return u2f(s[0]) + u2f(s[1]); }
__device__ __forceinline__ float x16_sum(float x) { return t16_sum(x, x); }
__device__ __forceinline__ float x32_sum(float x) { return t32_sum(x, x); }
__device__ __forceinline__ v4u sparse(unsigned v, const v4u& msk) { return v4u{v & msk[0], v & msk[1], v & msk[2], v & msk[3]}; }
__device__ __forceinline__ unsigned tag8(float v, unsigned T) { return perm(f2u(v), T, 0x07060500u); }

__device__ __forceinline__ void wg_barrier() { LGKM0(); __builtin_amdgcn_s_barrier(); MEMFENCE(); }
__device__ __forceinline__ void lds_wait_ge(LAS unsigned char* L, int word, unsigned want) {
#pragma nounroll
  for (unsigned spins = 0; spins < (SPIN_CAP << 4); ++spins) {
    if ((unsigned)__builtin_amdgcn_readfirstlane(*(volatile LAS unsigned*)(L + L_FLAG + word * 4)) >= want) break;
    __builtin_amdgcn_s_sleep(1);
  }
  MEMFENCE();
}
template <int WORD> __device__ __forceinline__ void lds_count(LAS unsigned char* L) {
  asm volatile("s_mov_b64 exec, 1\n\tds_add_u32 %0, %1 offset:%2\n\ts_mov_b64 exec, -1" ::"v"((unsigned)(size_t)L), "v"(1u), "n"(L_FLAG + WORD * 4) : "memory");
}
__device__ __forceinline__ unsigned now32() { return (unsigned)__builtin_amdgcn_s_memrealtime(); }

struct Stream {
  rsrc_t ka, kb, va, vb, wo;
  int kvo, vvo, ovo;
};
constexpr int aoff(int k) { return ((k & 3) << 10) | ((k >> 2) << 17); }
constexpr int boff(int k) { return ((k & 3) << 10) | (((k >> 2) & 3) << 16) | ((k >> 4) << 20); }
template <int K> __device__ __forceinline__ v4u issue(const Stream& S) {
  if constexpr (K < 8) return ld128<AUX_NT>(S.ka, S.kvo, aoff(K));
  else if constexpr (K < 40) return ld128<AUX_NT>(S.kb, S.kvo, boff(K - 8));
  else if constexpr (K < 48) return ld128<AUX_NT>(S.va, S.vvo, aoff(K - 40));
  else if constexpr (K < 80) return ld128<AUX_NT>(S.vb, S.vvo, boff(K - 48));
  else return ld128<AUX_NT>(S.wo, S.ovo, (K - 80) * 8 * 1152);
}
constexpr int id_at(int pos) { return pos < 8 ? pos : pos < 16 ? pos + 32 : pos < 48 ? pos - 8 : pos; }

struct State {
  v4u buf[84];
  v4u q[2];
  v4u msk;
  f32x4 d[4];
  float sc[10];
  unsigned pp[10];
  f32x4 acc[2];
  bool p1, p2;
  unsigned mode, mnext;
};

template <int LO, int HI> __device__ __forceinline__ void softmax(State& st, LAS unsigned char* L, int br, int wave, int lane) {
  float m = st.sc[LO];
#pragma unroll
  for (int t = LO + 1; t < HI; ++t) m = fmaxf(m, st.sc[t]);
  m = fmaxf(m, dpp<XOR1>(m)); m = fmaxf(m, dpp<XOR2>(m)); m = x16_max(m); m = x32_max(m);
  float l = 0.f;
#pragma unroll
  for (int t = LO; t < HI; ++t) {
    const float p = ex2(st.sc[t] - m);
    l += p;
    st.pp[t] = cvtpk(p, p);
  }
  l += dpp<XOR1>(l); l += dpp<XOR2>(l); l = x16_sum(l); l = x32_sum(l);
  *(LAS v2u*)(L + L_ML + ((br * 2 + ((lane >> 3) & 1)) * 8 + wave) * 8) = v2u{f2u(m), f2u(l)};
}

template <int K> __device__ __forceinline__ void process(State& st, LAS unsigned char* L, int wave, int lane) {
  if constexpr (K < 40) {
    constexpr int a = K & 3, t = K >> 2, br = t < 2 ? 0 : 1;
    st.d[a] = mfma(st.buf[K], st.q[br], f32x4{0.f, 0.f, 0.f, 0.f});
    if constexpr (a == 3) {
      float w[4];
#pragma unroll
      for (int x = 0; x < 4; ++x) {
        const float lo = st.p1 ? st.d[1 ^ x][1] : st.d[0 ^ x][0];
        const float hi = st.p1 ? st.d[3 ^ x][3] : st.d[2 ^ x][2];
        w[x] = st.p2 ? hi : lo;
      }
      const float r = (w[0] + dpp<XOR1>(w[1])) + (dpp<XOR2>(w[2]) + dpp<XOR3>(w[3]));
      st.sc[t] = rbf(rbf(r) * 0.08838834764831845f);
      if constexpr (t == 1) softmax<0, 2>(st, L, 0, wave, lane);
      if constexpr (t == 9) { softmax<2, 10>(st, L, 1, wave, lane); lds_count<2>(L); }
    }
  } else {
    constexpr int a = K & 3, t = (K - 40) >> 2, br = t < 2 ? 0 : 1;
    st.acc[br] = mfma(sparse(dppu<a * 0x55>(st.pp[t]), st.msk), st.buf[K], st.acc[br]);
    if constexpr (K == 48) { *(LAS f32x4*)(L + L_OP + wave * 1024 + lane * 16) = st.acc[0]; lds_count<3>(L); }
  }
}
template <int P, int HI> __device__ __forceinline__ void proc_range(State& st, LAS unsigned char* L, int wave, int lane) {
  if constexpr (P < HI) {
    process<id_at(P)>(st, L, wave, lane);
    proc_range<P + 1, HI>(st, L, wave, lane);
  }
}
template <int I> __device__ __forceinline__ void step(State& st, const Stream& S, LAS unsigned char* L, int wave) {
  asm volatile("s_cmp_lt_u32 %0, 2\n\ts_cbranch_scc1 1f\n\ts_waitcnt vmcnt(%2)\n1:\n\ts_cmp_lt_u32 %0, 1\n\ts_cbranch_scc1 2f\n\ts_waitcnt vmcnt(%1)\n2:"
               ::"s"(st.mode), "n"(W_NORM - 1), "n"(W_SLOW - 1) : "scc", "memory");
  VMWAIT(W_FAST - 1);
  st.buf[id_at(I)] = issue<id_at(I)>(S);
  SB();
  if constexpr ((I & 1) == 0 && I <= PACE_END) {
    st.mode = __builtin_amdgcn_readfirstlane(st.mnext);
    *(volatile LAS unsigned*)(L + L_PROG + wave * 4) = I;
    st.mnext = *(volatile LAS unsigned*)(L + L_MODE + wave * 4);
  }
}
template <int I, int HI> __device__ __forceinline__ void fill(State& st, const Stream& S, LAS unsigned char* L, int wave) {
  if constexpr (I < HI) {
    step<I>(st, S, L, wave);
    SB();
    fill<I + 1, HI>(st, S, L, wave);
  }
}
constexpr int cmin(int a, int b) { return a < b ? a : b; }
template <int I, int P> __device__ __forceinline__ void mainloop(State& st, const Stream& S, LAS unsigned char* L, int wave, int lane) {
  if constexpr (I < 80) {
    step<I>(st, S, L, wave);
    constexpr int PH = cmin(P + 4, I + 1 - W_FAST);
    proc_range<P, PH>(st, L, wave, lane);
    SB();
    mainloop<I + 1, PH>(st, S, L, wave, lane);
  } else if constexpr (P < 80) {
    if constexpr (P == 78) { VMWAIT(1); st.buf[80] = issue<80>(S); SB(); }
    proc_range<P, P + 1>(st, L, wave, lane);
    SB();
    mainloop<I, P + 1>(st, S, L, wave, lane);
  }
}

__global__ void __attribute__((aligned(4096))) __launch_bounds__(NTHR, 3)
fused_decode(const void*, const void*, const void*, const void*, const void*, const void*, void*, void*, const void*, const void*, const void*, const void*) {
  v8p pa; v2p pb;
  asm volatile("s_load_dwordx16 %0, %2, 0x0\n\ts_load_dwordx4 %1, %2, %3" : "=&s"(pa), "=&s"(pb)
               : "s"(__builtin_amdgcn_kernarg_segment_ptr()), "s"(64 + (blockIdx.x & 4) * 4) : "memory");
  SB();
  __shared__ __attribute__((aligned(16))) unsigned char smem[LDS_BYTES];
  LAS unsigned char* L = (LAS unsigned char*)smem;
  const int tid = threadIdx.x, lane = tid & 63, wave = __builtin_amdgcn_readfirstlane(tid >> 6);
  unsigned cp0 = 0, cp1 = 0, cp2 = 0;
  if (wave == 0) {
    unsigned long long pc; asm volatile("s_getpc_b64 %0" : "=s"(pc));
    const rsrc_t rc = mk((const void*)(pc & ~127ull), CODE_BYTES);
    cp0 = __builtin_amdgcn_raw_buffer_load_b32(rc, lane * 128, 0, 0); cp1 = __builtin_amdgcn_raw_buffer_load_b32(rc, lane * 128, 8192, 0);
    cp2 = __builtin_amdgcn_raw_buffer_load_b32(rc, lane * 128, 16384, 0);
  }
  SB();
  const int bid = blockIdx.x, w = (bid & 7) * 32 + (bid >> 3);
  const int pair = w >> 3, s = w & 7, b = pair >> 4, kvh = pair & 15;
  const int g = lane >> 4, n = lane & 15, j0 = n & 7;
  const int v0 = 9 * (w & 31), v = v0 + wave, urb = (w & 96) + (v & 31);
  unsigned offa = (pair * SK + SK - WIN) * (DH * 2) + ((s & 3) << 15) + ((s >> 2) << 18), offb = pair * SK * (DH * 2) + ((s & 1) << 15) + ((s >> 1) << 18);
  unsigned offw = urb * (32 * DM * 2) + (v >> 5) * 128, offq = (s & 1) * (288 * 8192) + kvh * 512 + (s >> 1) * 128;
  int qvo = (wave * 32 + g * 8 + (n >> 3) * 4) * 8192 + j0 * 16, xvo = ((n >> 3) * DM + (s & 1) * 288 + wave * 32 + g * 8) * 2;
  unsigned tk = (pair << 21) + 0x180000 + pair * 4352, tw0 = (w & 96) * (32 * DM * 2), tw1 = tw0 + 32 * (32 * DM * 2) - 128;
  PINS(offa); PINS(offb); PINS(offw); PINS(offq); PINV(qvo); PINV(xvo); PINS(tk); PINS(tw0); PINS(tw1);
  unsigned long long t64;
  SB();
  asm volatile("s_memrealtime %2\n\ts_waitcnt lgkmcnt(0)" : "+s"(pa), "+s"(pb), "=&s"(t64)::"memory");
  SB();
  const unsigned t_start = (unsigned)t64;
  typedef const char* P;
  const P x = (P)pa[0], kA = (P)pa[1], vA = (P)pa[2], kB = (P)pa[3], vB = (P)pa[4], gw = (P)pa[5], ws = (P)pa[7], wqb = (P)pb[0], wob = (P)pb[1];
  unsigned e0, d1, d2, d3, d4;
  asm volatile("s_cmp_lg_u32 %16, 9\n\ts_cbranch_scc1 1f\n\t"
               "s_cmp_lg_u32 %17, 0\n\ts_cbranch_scc1 2f\n\t"
               "s_load_dword %1, %6, %13\n\ts_load_dword %2, %7, %13\n\ts_load_dword %3, %8, %13\n\ts_load_dword %4, %9, %13\n"
               "2:\n\ts_cmp_lg_u32 %17, 1\n\ts_cbranch_scc1 3f\n\t"
               "s_load_dword %1, %10, 0x0\n\ts_load_dword %2, %12, %14\n\ts_load_dword %3, %12, %15\n\ts_load_dword %4, %11, 0x0\n"
               "3:\n\ts_load_dword %0, %5, 0x0\n\ts_barrier\n1:"
               : "=&s"(e0), "=&s"(d1), "=&s"(d2), "=&s"(d3), "=&s"(d4)
               : "s"(ws), "s"(kA), "s"(vA), "s"(kB), "s"(vB), "s"(x), "s"(gw), "s"(wob), "s"(tk), "s"(tw0), "s"(tw1), "s"(wave), "s"(s)
               : "scc", "memory");

  if (wave == 9) {
    asm volatile("s_waitcnt lgkmcnt(0)" : "+s"(e0), "+s"(d1), "+s"(d2), "+s"(d3), "+s"(d4)::"memory");
    const rsrc_t rws = mk(ws, WS_END);
    const unsigned tg = (e0 + 1u) & 0xffffu;
    volatile LAS unsigned* prog = (volatile LAS unsigned*)(L + L_PROG) + j0;
    volatile LAS unsigned* modes = (volatile LAS unsigned*)(L + L_MODE) + j0;
    int ref = 0; unsigned tref = 0; bool have = false;
    unsigned until = ((t_start >> 8) + 1) << 8;
#pragma nounroll
    for (unsigned spins = 0; spins < SPIN_CAP; ++spins) {
      const int p = *prog;
      if (__any(p >= PACE_END)) break;
      const unsigned now = now32();
      if (have) {
        const int lead = (short)(p * (2 * TAU) - 2 * now - ref);
        *modes = lead > LEAD_HI ? 2u : (lead < -LEAD_LO ? 0u : 1u);
      }
      if ((int)(now - until) >= 0) {
        const int slot = WS_PACE + ((until >> 8) & 31) * 1024;
        if (until & 128) {
          MEMFENCE();
          const unsigned o = __builtin_amdgcn_raw_buffer_load_b32(rws, j0 * 128, slot, AUX_SC1);
          if (__all((o & 0xffffu) == tg)) {
            const int b0 = __builtin_amdgcn_readfirstlane(o >> 16);
            int d = (short)((o >> 16) - b0);
            d += dppu<XOR1>(d); d += dppu<XOR2>(d); d += dppu<XOR7>(d);
            ref = b0 + (d >> 3); tref = until - 128; have = true;
          }
        } else if ((bid >> 3) == PUB) {
          int sum = p + dppu<XOR1>(p); sum += dppu<XOR2>(sum); sum += dppu<XOR7>(sum);
          __builtin_amdgcn_raw_buffer_store_b32((((sum * TAU - 8 * now) >> 2) << 16) | tg, rws, lane ? OOB : (bid & 7) * 128, slot, AUX_SC1);
        }
        until += 128;
      } else __builtin_amdgcn_s_sleep(4);
    }
    const int rem = (short)(80 * 2 * TAU - __builtin_amdgcn_readfirstlane(ref) - 2 * tref);
    const unsigned due = have ? tref + ((rem * 243) >> 9) + DUE_OB : now32();
    *(volatile LAS unsigned*)(L + L_FLAG + 20) = due;
    wg_barrier();
    if (b) {
      const unsigned te = now32();
#pragma nounroll
      for (unsigned spins = 0; spins < 64; ++spins) {
        const unsigned now = now32();
        if ((int)(now - due) >= 0 && (int)(now - te) >= DUE_OWN) break;
        __builtin_amdgcn_s_sleep(1);
      }
    }
    wg_barrier();
    ENDWAVE();
  }

  const rsrc_t rwq = mk(wqb + offq, 287 * 8192 + 128);
  v4u wq[4];
#pragma unroll
  for (int i = 0; i < 4; ++i) wq[i] = ld128<AUX_NT>(rwq, qvo, i * 8192);
  const v4u xr = ld128<0>(mk(x, 2 * DM * 2), xvo, 0);
  const unsigned epv = __builtin_amdgcn_raw_buffer_load_b32(mk(ws, wave == 8 ? 4 : 0), 0, 0, AUX_SC1);
  SB();
  const P kap = kA + offa, wop = wob + offw;
  constexpr int A_SPAN = aoff(7) + 7 * 4096 + 1024, B_SPAN = boff(31) + 7 * 4096 + 1024, WO_SPAN = 31 * 1152 + 128;
  Stream S;
  S.kvo = wave * 4096 + (n >> 2) * 256 + g * 64 + (n & 3) * 16;
  S.ovo = (lane >> 3) * 1152 + j0 * 16;
  State st;
  if (tid >= 512 && tid < 536) *(LAS unsigned*)(L + L_FLAG + lane * 4) = lane < 16 ? 0u : 1u;
  {
    const rsrc_t r0 = mk(wave < 8 ? kap : wop, wave < 8 ? A_SPAN : WO_SPAN);
    const int vo0 = wave < 8 ? S.kvo : S.ovo, st0 = wave < 8 ? 1024 : 8 * 1152;
    SB(); wg_barrier(); SB();
#pragma unroll
    for (int i = 0; i < NB0; ++i) st.buf[i] = ld128<AUX_NT>(r0, vo0, i * st0);
  }
  SB();
  const rsrc_t rws = mk(ws, WS_END);
  S.ka = mk(kap, A_SPAN); S.va = mk(vA + offa, A_SPAN);
  S.kb = mk(kB + offb, B_SPAN); S.vb = mk(vB + offb, B_SPAN);
  S.wo = mk(wop, WO_SPAN);
  S.vvo = wave * 4096 + lane * 16;
#pragma unroll
  for (int i = 0; i < 4; ++i) st.msk[i] = ((j0 >> 1) == i) ? ((j0 & 1) ? 0xffff0000u : 0x0000ffffu) : 0u;
  st.p1 = lane & 1; st.p2 = lane & 2;
  {
    f32x4 d0 = {0.f, 0.f, 0.f, 0.f}, d1 = {0.f, 0.f, 0.f, 0.f};
#pragma unroll
    for (int i = 0; i < 4; ++i) {
      const unsigned sel = (i & 1) ? 0x03020302u : 0x01000100u;
      d0 = mfma(sparse(perm(xr[i >> 1], xr[i >> 1], sel), st.msk), wq[i], d0);
      d1 = mfma(sparse(perm(xr[2 + (i >> 1)], xr[2 + (i >> 1)], sel), st.msk), wq[i], d1);
    }
    f32x4 y;
#pragma unroll
    for (int i = 0; i < 4; ++i) y[i] = d0[i] + dpp<ROR8>(d1[i]);
    *(LAS f32x4*)(L + L_QP + (n < 8 ? wave : 9) * 512 + (g >> 1) * 256 + j0 * 32 + (g & 1) * 16) = y;
    lds_count<0>(L);
    asm volatile("" ::"v"(cp0), "v"(cp1), "v"(cp2));
    SB();
  }
  unsigned ep, T, T16;
  auto wo_frags = [&](const v4u w0, const v4u w1, const v4u w2, const v4u w3, v4u (&bf)[4]) INL {
    LAS unsigned char* wr = L + L_WO + wave * 4608 + (lane >> 3) * 144 + j0 * 16;
    *(LAS v4u*)(wr) = w0; *(LAS v4u*)(wr + 8 * 144) = w1; *(LAS v4u*)(wr + 16 * 144) = w2; *(LAS v4u*)(wr + 24 * 144) = w3;
    MEMFENCE();
    LAS unsigned char* rd = L + L_WO + wave * 4608 + (g * 8 + (n >> 2)) * 144 + (n & 3) * 8;
#pragma unroll
    for (int t = 0; t < 4; ++t) {
      const v2u lo = ldtr(rd + t * 32), hi = ldtr(rd + 4 * 144 + t * 32);
      bf[t] = v4u{lo[0], lo[1], hi[0], hi[1]};
    }
    SB();
  };
  int ovo = ((urb >> 3) * 8 + (lane >> 3)) * MBS + (b ? WS_OB + (urb & 7) * 128 + j0 * 16 : WS_OA + (urb & 7) * 64 + (j0 >> 1) * 16);
  unsigned psel = b ? 0x07060302u : (j0 & 1) ? 0x07060504u : 0x03020100u, tmask = b ? 0xffffu : 0u;
  int awa = L_A + wave * 1280 + (lane >> 3) * 80 + j0 * 8, ara = L_A + wave * 1280 + n * 80 + g * 16;
  int wra = L_W + (((v >> 3) - (v0 >> 3)) * 2 + ((urb >> 2) & 1)) * 64 + g * 16;
  const int cut = 32 - (v0 & 31);
  int pwa = L_PP + (wave >= cut ? 4608 : 0) + wave * 512 + (lane >> 5) * 256 + (lane & 31) * 4;
  auto gather = [&](v4u& o0, v4u& o1) INL { o0 = ld128<AUX_SC1>(rws, ovo, 0); o1 = ld128<AUX_SC1>(rws, ovo, 128 * MBS); };
  auto project = [&](const v4u (&bf)[4], v4u o0, v4u o1) INL {
    const f32x4 wt = *(LAS f32x4*)(L + wra);
    SB();
    v4u a;
    auto take = [&]() INL -> bool {
      *(LAS v2u*)(L + awa) = v2u{perm(o0[2], o0[0], psel), perm(o0[3], o0[1], psel)};
      *(LAS v2u*)(L + awa + 640) = v2u{perm(o1[2], o1[0], psel), perm(o1[3], o1[1], psel)};
      MEMFENCE();
      a = *(LAS v4u*)(L + ara);
      SB();
      const unsigned b0 = __builtin_amdgcn_bitop3_b32(o0[0], o0[1], T16, 0x7e), b1 = __builtin_amdgcn_bitop3_b32(o0[2], o0[3], T16, 0x7e);
      const unsigned b2 = __builtin_amdgcn_bitop3_b32(o1[0], o1[1], T16, 0x7e), b3 = __builtin_amdgcn_bitop3_b32(o1[2], o1[3], T16, 0x7e);
      return __all(((b0 | b1 | b2 | b3) & tmask) == 0);
    };
    unsigned spins = SPIN_CAP;
    bool ok = take();
#pragma nounroll
    while (!ok) {
      __builtin_amdgcn_s_sleep(1);
      MEMFENCE();
      gather(o0, o1);
      ok = take() | (--spins == 0);
    }
    f32x4 d[4];
#pragma unroll
    for (int t = 0; t < 4; ++t) d[t] = mfma(a, bf[t], f32x4{0.f, 0.f, 0.f, 0.f});
    SB();
    float sm[4];
#pragma unroll
    for (int t = 0; t < 4; ++t) { const f32x2 h = f32x2{wt[0], wt[1]} * f32x2{d[t][0], d[t][1]} + f32x2{wt[2], wt[3]} * f32x2{d[t][2], d[t][3]}; sm[t] = h[0] + h[1]; }
    *(LAS float*)(L + pwa) = t16_sum(sm[0], sm[1]);
    *(LAS float*)(L + pwa + 128) = t16_sum(sm[2], sm[3]);
  };
  auto publish_p = [&](const int pra, const int pxo) INL {
    f32x2 p[9];
#pragma unroll
    for (int j = 0; j < 9; ++j) p[j] = *(LAS f32x2*)(L + pra + j * 512);
    const f32x2 y = ((p[0] + p[1]) + (p[2] + p[3])) + ((p[4] + p[5]) + (p[6] + p[7] + p[8]));
    __builtin_amdgcn_raw_buffer_store_b64(v2u{tag8(y[0], T), tag8(y[1], T)}, rws, pxo, 0, AUX_SC1);
  };

  if (__builtin_expect(wave < 8, 1)) {
    st.mode = 1; st.mnext = 1;
    fill<NB0, N_FILL>(st, S, L, wave);
    lds_wait_ge(L, 1, 1u);
    ep = __builtin_amdgcn_readfirstlane(*(LAS unsigned*)(L + L_FLAG + 16)); T = ep & 0xffu; T16 = ep & 0xffffu;
    st.q[0] = *(LAS v4u*)(L + L_Q + (n >> 3) * 256 + (g * 4 + (n & 3)) * 16);
    st.q[1] = *(LAS v4u*)(L + L_Q + 512 + (n >> 3) * 256 + (g * 4 + (n & 3)) * 16);
    st.acc[0] = f32x4{0.f, 0.f, 0.f, 0.f}; st.acc[1] = f32x4{0.f, 0.f, 0.f, 0.f};
    int pra = L_PP + 4608 + lane * 8, pxo = WS_PX + w * MBS + 512 + lane * 8;
    PINV(ovo); PINV(pwa); PINV(T16); PINV(awa); PINV(ara); PINV(wra); PINV(psel); PINV(tmask); PINV(pra); PINV(pxo);
    mainloop<N_FILL, 0>(st, S, L, wave, lane);
    *(LAS f32x4*)(L + L_OP + 8192 + wave * 1024 + lane * 16) = st.acc[1];
    SB();
    st.buf[81] = issue<81>(S); st.buf[82] = issue<82>(S); st.buf[83] = issue<83>(S);
    SB();
    wg_barrier();
    wg_barrier();
    v4u bf[4], o0, o1;
    gather(o0, o1);
    SB();
    wo_frags(st.buf[80], st.buf[81], st.buf[82], st.buf[83], bf);
    project(bf, o0, o1);
    wg_barrier();
    if (wave == 0 && cut < 9) publish_p(pra, pxo);
    return;
  }

  __builtin_amdgcn_s_setprio(3);
  static_assert(NB0 == 2, "the rest of the control wave's wo tile");
  st.buf[2] = issue<82>(S); st.buf[3] = issue<83>(S);
  ep = __builtin_amdgcn_readfirstlane(epv) + 1u;
  T = ep & 0xffu; T16 = ep & 0xffffu;
  *(LAS unsigned*)(L + L_FLAG + 16) = ep;
  v4u bf[4];
  lds_wait_ge(L, 0, 9u);
  {
    f32x4 qs = *(LAS f32x4*)(L + L_QP + (lane & 31) * 16);
#pragma unroll
    for (int i = 1; i < 9; ++i) qs += *(LAS f32x4*)(L + L_QP + i * 512 + (lane & 31) * 16);
    qs = __builtin_bit_cast(f32x4, __builtin_bit_cast(v4u, qs) + 0x80u);
    st128(v4u{tag8(qs[0], T), tag8(qs[1], T), tag8(qs[2], T), tag8(qs[3], T)}, rws, lane < 32 ? lane * 16 : OOB, WS_QX + w * MBS);
  }
  wo_frags(st.buf[0], st.buf[1], st.buf[2], st.buf[3], bf);
  PINV(bf[0]); PINV(bf[1]); PINV(bf[2]); PINV(bf[3]);
#pragma nounroll
  for (unsigned spins = 0; spins < 4096 && (int)(now32() - t_start) < DLY_Q; ++spins) __builtin_amdgcn_s_sleep(2);
  {
    v4u qx[4];
    const int gvo = g * 2 * MBS + n * 16, gso = WS_QX + kvh * 8 * MBS + b * 256;
#pragma nounroll
    for (unsigned spins = 0; spins < SPIN_CAP; ++spins) {
      MEMFENCE();
#pragma unroll
      for (int i = 0; i < 4; ++i) qx[i] = ld128<AUX_SC1>(rws, gvo, gso + ((i >> 1) * 128 + (i & 1)) * MBS) ^ T;
      const v4u bad = qx[0] | qx[1] | qx[2] | qx[3];
      if (__all(((bad[0] | bad[1] | bad[2] | bad[3]) & 0xffu) == 0)) break;
      __builtin_amdgcn_s_sleep(16);
    }
#pragma unroll
    for (int br = 0; br < 2; ++br) {
      const f32x4 v = __builtin_bit_cast(f32x4, qx[br * 2]) + __builtin_bit_cast(f32x4, qx[br * 2 + 1]);
      *(LAS v2u*)(L + L_Q + br * 512 + lane * 8) = v2u{cvtpk(v[0], v[1]), cvtpk(v[2], v[3])};
    }
    lds_count<1>(L);
  }
  float gt = 0.f;
  int rvo[5] = {OOB, OOB, OOB, OOB, OOB};
  if (w < 72) {
    const rsrc_t rg = mk(gw, DM * 2), rxr = mk(x + ((w >> 2) & 1) * (DM * 2), DM * 2);
    float z = 0.f;
#pragma unroll
    for (int i = 0; i < 5; ++i) {
      const unsigned gg = __builtin_amdgcn_raw_buffer_load_b32(rg, (i * 64 + lane) * 4, 0, 0), xx = __builtin_amdgcn_raw_buffer_load_b32(rxr, (i * 64 + lane) * 4, 0, 0);
      z += rbf(u2f(xx << 16) * u2f(gg << 16)) + rbf(u2f(xx & 0xffff0000u) * u2f(gg & 0xffff0000u));
    }
    z = x32_sum(z); z = x16_sum(z); z += dpp<ROR8>(z); z += dpp<ROR4>(z); z += dpp<XOR2>(z); z += dpp<XOR1>(z);
    gt = rbf(1.f / (1.f + __expf(-rbf(z))));
    gt = (lane & 32) ? rbf(1.f - gt) : gt;
#pragma unroll
    for (int i = 0; i < 5; ++i) {
      const int c = w >> 3, j = (32 * c) / 9 + i;
      rvo[i] = 9 * j <= 32 * c + 31 ? WS_PX + ((lane >> 3) * 32 + j) * MBS + (((9 * j) >> 5) == c ? 0 : 512) + (w & 7) * 64 + j0 * 8 : OOB;
    }
  }
  unsigned tv = T, t4 = rvo[4] == OOB ? 0u : T;
#pragma unroll
  for (int i = 0; i < 5; ++i) PINV(rvo[i]);
  PINV(tv); PINV(t4);
  PINV(gt);
  lds_wait_ge(L, 2, 8u);
  v2u mlw;
  {
    const v2u ml = *(LAS v2u*)(L + L_ML + (lane & 31) * 8);
    const float m = u2f(ml[0]);
    float M = fmaxf(m, dpp<XOR1>(m)); M = fmaxf(M, dpp<XOR2>(M)); M = fmaxf(M, dpp<XOR7>(M));
    const float e = ex2(m - M);
    float Ls = e * u2f(ml[1]);
    PINV(Ls);
    Ls += dpp<XOR1>(Ls); Ls += dpp<XOR2>(Ls); Ls += dpp<XOR7>(Ls);
    Ls = u2f(f2u(Ls) & 0xffffff00u);
    *(LAS float*)(L + L_F + (lane & 31) * 4) = e / Ls;
    mlw = v2u{(f2u(M) & 0xffff0000u) | T16, f2u(Ls) | T};
    MEMFENCE();
  }
  auto combine = [&](const int br, const f32x4 f0, const f32x4 f1) INL -> f32x4 {
    f32x4 op[8];
#pragma unroll
    for (int i = 0; i < 8; ++i) op[i] = *(LAS f32x4*)(L + L_OP + br * 8192 + i * 1024 + lane * 16);
    f32x4 O = f0[0] * op[0];
    O += f0[1] * op[1]; O += f0[2] * op[2]; O += f0[3] * op[3];
    O += f1[0] * op[4]; O += f1[1] * op[5]; O += f1[2] * op[6]; O += f1[3] * op[7];
    return O;
  };
  const int dpos = (g >> 1) * 128 + n * 8 + (g & 1) * 4, fa = L_F + (g >> 1) * 32;
  lds_wait_ge(L, 3, 8u);
  {
    const f32x4 O = combine(0, *(LAS f32x4*)(L + fa), *(LAS f32x4*)(L + fa + 16));
    __builtin_amdgcn_raw_buffer_store_b64(v2u{cvtpk(O[0], O[1]), cvtpk(O[2], O[3])}, rws, dpos * 2, WS_OA + w * MBS, AUX_SC1);
    asm volatile("s_waitcnt vmcnt(0)" ::: "memory");
    __builtin_amdgcn_raw_buffer_store_b64(mlw, rws, (lane & 0x27) ? OOB : lane, WS_ML + w * MBS, AUX_SC1);
  }
  f32x4 fb0 = *(LAS f32x4*)(L + fa + 64), fb1 = *(LAS f32x4*)(L + fa + 80);
  if (cut < 9) {
#pragma unroll
    for (int j = 0; j < 9; ++j) *(LAS v4u*)(L + L_PP + j * 1024 + lane * 16) = v4u{0u, 0u, 0u, 0u};
  }
  int svo = WS_ML + ((g & 1) * 128 + (w & 96) + ((v0 + (lane >> 5) * 8) & 24) + ((lane >> 1) & 7)) * MBS + b * 16 + (lane & 1) * 8;
  int wwa = L_W + (((lane >> 5) * 2 + (lane & 1)) * 16 + ((lane >> 1) & 15)) * 4;
  int obo = WS_OB + w * MBS + dpos * 4, pxo = WS_PX + w * MBS + lane * 8, pra = L_PP + lane * 8;
  GAS unsigned short* op = (GAS unsigned short*)pa[6] + ((w >> 2) & 1) * DM + (w >> 3) * 64 + (w & 3) * 16 + j0 * 2 + (g & 1);
  unsigned sl = 0x05040100u, sh = 0x07060100u;
  PINV(awa); PINV(ara); PINV(wra); PINV(psel); PINV(tmask); PINV(ovo); PINV(pwa); PINV(T16);
  PINV(fb0); PINV(fb1); PINV(obo); PINV(pxo); PINV(pra); PINV(op); PINV(svo); PINV(wwa); PINV(sl); PINV(sh);
  lds_wait_ge(L, L_PROG / 4, STAT_AT);
  v2u sx = __builtin_amdgcn_raw_buffer_load_b64(rws, svo, 0, AUX_SC1);
  {
    sx ^= v2u{T16, T};
    unsigned spins = SPIN_CAP;
    bool ok = __all(((sx[0] & 0xffffu) | (sx[1] & 0xffu)) == 0);
#pragma nounroll
    while (!ok) {
      __builtin_amdgcn_s_sleep(8);
      MEMFENCE();
      sx = __builtin_amdgcn_raw_buffer_load_b64(rws, svo, 0, AUX_SC1) ^ v2u{T16, T};
      ok = __all(((sx[0] & 0xffffu) | (sx[1] & 0xffu)) == 0) | (--spins == 0);
    }
    const float m = u2f(sx[0]);
    float M = fmaxf(m, dpp<XOR2>(m)); M = fmaxf(M, dpp<ROR4>(M)); M = fmaxf(M, dpp<ROR8>(M));
    float wg = ex2(m - M) * u2f(sx[1]);
    PINV(wg);
    float Lg = wg + dpp<XOR2>(wg); Lg += dpp<ROR8>(Lg); Lg += dpp<ROR4>(Lg);
    *(LAS float*)(L + wwa) = wg * __builtin_amdgcn_rcpf(Lg);
  }
  VMWAIT(0);
  wg_barrier();
  {
    const f32x4 O = combine(1, fb0, fb1);
    const unsigned c0 = cvtpk(O[0], O[1]), c1 = cvtpk(O[2], O[3]);
    st128(v4u{perm(c0, T16, sl), perm(c1, T16, sl), perm(c0, T16, sh), perm(c1, T16, sh)}, rws, obo, 0);
  }
  wg_barrier();
  { v4u o0, o1; gather(o0, o1); SB(); project(bf, o0, o1); }
  wg_barrier();
  publish_p(pra, pxo);
  if (w >= 72) ENDWAVE();
  {
    v2u px[5], a0 = {0u, 0u}, a1 = a0, a2 = a0, a3 = a0, a4 = a0, b0 = a0, b1 = a0, b2 = a0, b3 = a0, b4 = a0;
    const v4u rd = {(unsigned)(size_t)ws, (unsigned)((size_t)ws >> 32) & 0xffffu, (unsigned)WS_END, 0x00020000u};
#define POLL5(r0, r1, r2, r3, r4) asm volatile("v_mov_b64 %0, 0\n\tv_mov_b64 %1, 0\n\tv_mov_b64 %2, 0\n\tv_mov_b64 %3, 0\n\tv_mov_b64 %4, 0\n\t" \
      "buffer_atomic_add_x2 %0, %5, %10, 0 offen sc0 sc1\n\tbuffer_atomic_add_x2 %1, %6, %10, 0 offen sc0 sc1\n\t" \
      "buffer_atomic_add_x2 %2, %7, %10, 0 offen sc0 sc1\n\tbuffer_atomic_add_x2 %3, %8, %10, 0 offen sc0 sc1\n\t" \
      "buffer_atomic_add_x2 %4, %9, %10, 0 offen sc0 sc1" \
      : "+v"(r0), "+v"(r1), "+v"(r2), "+v"(r3), "+v"(r4) : "v"(rvo[0]), "v"(rvo[1]), "v"(rvo[2]), "v"(rvo[3]), "v"(rvo[4]), "s"(rd) : "memory")
#define TAKE5(r0, r1, r2, r3, r4) asm volatile("s_waitcnt vmcnt(5)" : "+v"(r0), "+v"(r1), "+v"(r2), "+v"(r3), "+v"(r4)::"memory"); \
      px[0] = r0 ^ tv; px[1] = r1 ^ tv; px[2] = r2 ^ tv; px[3] = r3 ^ tv; px[4] = r4 ^ t4; \
      if (__all(((px[0][0] | px[0][1] | px[1][0] | px[1][1] | px[2][0] | px[2][1] | px[3][0] | px[3][1] | px[4][0] | px[4][1]) & 0xffu) == 0)) break
    const unsigned due = __builtin_amdgcn_readfirstlane(*(volatile LAS unsigned*)(L + L_FLAG + 20)) + DUE_PX;
#pragma nounroll
    for (unsigned spins = 0; spins < 64 && (int)(now32() - due) < 0; ++spins) __builtin_amdgcn_s_sleep(1);
    POLL5(a0, a1, a2, a3, a4);
    __builtin_amdgcn_s_sleep(6);
#pragma nounroll
    for (unsigned spins = 0; spins < SPIN_CAP; ++spins) {
      POLL5(b0, b1, b2, b3, b4);
      TAKE5(a0, a1, a2, a3, a4);
      POLL5(a0, a1, a2, a3, a4);
      TAKE5(b0, b1, b2, b3, b4);
    }
#undef POLL5
#undef TAKE5
    const f32x2 y = (__builtin_bit_cast(f32x2, px[0]) + __builtin_bit_cast(f32x2, px[1])) + (__builtin_bit_cast(f32x2, px[2]) + __builtin_bit_cast(f32x2, px[3])) + __builtin_bit_cast(f32x2, px[4]);
    float ev = t16_sum(y[0], y[1]);
    ev += dpp<ROR8>(ev);
    const float r = x32_sum(rbf(gt * rbf(ev)));
    *op = (unsigned short)(cvtpk(r, r) & 0xffffu);
    if (w == 0) __builtin_amdgcn_raw_buffer_store_b32(ep, rws, lane ? OOB : 0, 0, AUX_SC1);
    asm volatile("s_waitcnt vmcnt(0)" : "+v"(a0), "+v"(a1), "+v"(a2), "+v"(a3), "+v"(a4), "+v"(b0), "+v"(b1), "+v"(b2), "+v"(b3), "+v"(b4)::"memory");
  }
  ENDWAVE();
}

extern "C" void kernel_launch(void* const* d_in, const int* in_sizes, int n_in, void* d_out, int out_size, void* d_ws, size_t ws_size,
                              hipStream_t stream) {
  fused_decode<<<256, NTHR, 0, stream>>>(d_in[0], d_in[1], d_in[2], d_in[6], d_in[7], d_in[10], d_out, d_ws, d_in[3], d_in[4], d_in[8], d_in[9]);
}
```
